```python
import torch

def _softmax(x, axis=-1):
    return torch.softmax(x, dim=axis)

def _gelu(x):
    return torch.nn.functional.gelu(x, approximate='tanh')

def _silu(x):
    return torch.nn.functional.silu(x)

def _sigmoid(x):
    return torch.sigmoid(x)

def _layernorm(x, axis=-1, eps=1e-05):
    m = x.mean(dim=axis, keepdim=True)
    v = x.var(dim=axis, keepdim=True, unbiased=False)
    return (x - m) / torch.sqrt(v + eps)

def _scatter_add(tgt, idx, src, axis=0):
    return tgt.clone().index_add_(axis, idx, src)

def _segment_sum(src, seg, n):
    out = torch.zeros((n,) + src.shape[1:], dtype=src.dtype)
    return out.index_add_(0, seg, src)

def _pad(x, pw):
    flat = tuple((v for pair in reversed(pw) for v in pair))
    return torch.nn.functional.pad(x, flat)
kernel_name = 'adaLN_cond__ssm_conv_s4__dense_activation_silu__prompt'

def setup_inputs():

    _w = lambda a: (lambda t: t.to(torch.bfloat16) if t.is_floating_point() else t)(torch.as_tensor(a))

    def _A_setup():
        B, L, D, N = (1, 2048, 576, 128)
        A_re = -(lambda _x: torch.exp(torch.as_tensor(_x)))((lambda *s: torch.randn(*s, dtype=torch.bfloat16) * 3.2)(D, N))
        A_im = torch.pi * (lambda *s: torch.randn(*s, dtype=torch.bfloat16) * 0.05)(D, N)
        B_re = (lambda *s: torch.randn(*s, dtype=torch.bfloat16) * 0.1)(D, N)
        B_im = (lambda *s: torch.randn(*s, dtype=torch.bfloat16) * 0.1)(D, N)
        C_re = (lambda *s: torch.randn(*s, dtype=torch.bfloat16) * 0.1)(D, N)
        C_im = (lambda *s: torch.randn(*s, dtype=torch.bfloat16) * 0.1)(D, N)
        log_dt = (lambda *s: torch.randn(*s, dtype=torch.bfloat16) * 0.05)(D) * 0.5 - 4.0
        Dskip = (lambda *s: torch.randn(*s, dtype=torch.bfloat16) * 0.05)(D)
        return {'u': _w((lambda *s: torch.randn(*s, dtype=torch.bfloat16) * 0.05)(B, L, D)), 'A_re': _w(A_re), 'A_im': _w(A_im), 'B_re': _w(B_re), 'B_im': _w(B_im), 'C_re': _w(C_re), 'C_im': _w(C_im), 'log_dt': _w(log_dt), 'Dskip': _w(Dskip)}
    _A_in = _A_setup()

    def _B_setup():
        B, S, D, F = (1, 2048, 576, 4608)
        return {'x': _w((lambda *s: torch.randn(*s, dtype=torch.bfloat16) * 0.05)(B, S, D)), 'w1': _w((lambda *s: torch.randn(*s, dtype=torch.bfloat16) * 3.2)(D, F) * D ** (-0.5)), 'w2': _w((lambda *s: torch.randn(*s, dtype=torch.bfloat16) * 0.4)(F, D) * F ** (-0.5))}
    _B_in = _B_setup()
    return {'A_u': _A_in['u'], 'A_A_re': _A_in['A_re'], 'A_A_im': _A_in['A_im'], 'A_B_re': _A_in['B_re'], 'A_B_im': _A_in['B_im'], 'A_C_re': _A_in['C_re'], 'A_C_im': _A_in['C_im'], 'A_log_dt': _A_in['log_dt'], 'A_Dskip': _A_in['Dskip'], 'B_x': _B_in['x'], 'B_w1': _B_in['w1'], 'B_w2': _B_in['w2'], 'c': _w((lambda *s: torch.randn(*s, dtype=torch.bfloat16) * 0.2)(1, 576) * 8.0), 'w_mod': _w((lambda *s: torch.randn(*s, dtype=torch.bfloat16) * 0.2)(576, 6 * 576) * 576 ** (-0.5) * 8.0)}

def reference(A_u, A_A_re, A_A_im, A_B_re, A_B_im, A_C_re, A_C_im, A_log_dt, A_Dskip, B_x, B_w1, B_w2, c, w_mod):

    def _A_ref(u, A_re, A_im, B_re, B_im, C_re, C_im, log_dt, Dskip):
        Bsz, L, D = u.shape
        dt = torch.exp(log_dt)[:, None]
        A = A_re + 1j * A_im
        Bc = B_re + 1j * B_im
        Cc = C_re + 1j * C_im
        logdA = dt * A
        dB = dt * Bc
        l = (lambda x, dt: x.to(dt))(torch.arange(L), A_re.dtype)
        P = torch.exp(logdA[:, :, None] * l[None, None, :])
        K = torch.einsum('dn,dnl,dn->dl', Cc, P, dB).real
        L2 = 2 * L
        Kp = _pad(K, ((0, 0), (0, L)))
        up = (lambda x, *d: x.permute(*d))(u, 0, 2, 1)
        up = _pad(up, ((0, 0), (0, 0), (0, L)))
        Kf = (lambda x, axes=(-2, -1): torch.fft.rfft2(x.float(), dim=axes))(Kp[:, None, :], axes=(-2, -1))
        Uf = (lambda x, axes=(-2, -1): torch.fft.rfft2(x.float(), dim=axes))(up[:, :, None, :], axes=(-2, -1))
        Yf = Uf * Kf[None, :, :, :]
        y = (lambda x, s, axes=(-2, -1): torch.fft.irfft2(x, s=s, dim=axes).to(torch.bfloat16))(Yf, (1, L2), axes=(-2, -1))[:, :, 0, :L]
        y = (lambda x, *d: x.permute(*d))(y, 0, 2, 1) + u * Dskip[None, None, :]
        return y

    def _B_ref(x, w1, w2):
        h = torch.einsum('bsd,df->bsf', x, w1)
        h = _silu(h)
        out = torch.einsum('bsf,fd->bsd', h, w2)
        return out
    x = A_u
    mod = (_silu(c) @ w_mod).reshape(c.shape[0], 6, 1, -1)
    sa, ca, ga, sb, cb, gb = (mod[:, 0].clone(), mod[:, 1].clone(), mod[:, 2].clone(), mod[:, 3].clone(), mod[:, 4].clone(), mod[:, 5].clone())
    h = _layernorm(x, axis=-1) * (1 + ca) + sa
    x = x + ga * _A_ref(h, A_A_re, A_A_im, A_B_re, A_B_im, A_C_re, A_C_im, A_log_dt, A_Dskip)
    h = _layernorm(x, axis=-1) * (1 + cb) + sb
    x = x + gb * _B_ref(h, B_w1, B_w2)
    return x
```

```cpp
#include <hip/hip_runtime.h>
#include <hip/hip_bf16.h>
#include <stdint.h>

typedef unsigned short u16;
using bf16x8 = __attribute__((ext_vector_type(8))) short;
using f32x4 = __attribute__((ext_vector_type(4))) float;
using s16x4 = __attribute__((ext_vector_type(4))) short;
typedef __attribute__((address_space(3))) s16x4* lds_s16x4_ptr;

constexpr int L = 2048, D = 576, NS = 128, F = 4608;
constexpr int KDS = 656;

__device__ __forceinline__ float bf2f(u16 v) { return __uint_as_float(((uint32_t)v) << 16); }
__device__ __forceinline__ u16 f2bf(float f) {
  uint32_t u = __float_as_uint(f);
  u += 0x7FFFu + ((u >> 16) & 1u);
  return (u16)(u >> 16);
}
__device__ __forceinline__ float rbf(float f) { return bf2f(f2bf(f)); }

__device__ __forceinline__ float wave_sum(float v) {
#pragma unroll
  for (int o = 32; o > 0; o >>= 1) v += __shfl_xor(v, o);
  return v;
}

__device__ __forceinline__ int kidx_of(int l) {
  int lv = (int)rbf((float)l);
  if (lv < 256) return lv;
  if (lv < 512) return 256 + ((lv - 256) >> 1);
  if (lv < 1024) return 384 + ((lv - 512) >> 2);
  return 512 + ((lv - 1024) >> 3);
}

constexpr int K0_MOD_BLOCKS = 54, K0_STAT_BLOCKS = 32;

__global__ __launch_bounds__(256) void k0_prep(const u16* __restrict__ c, const u16* __restrict__ wmod,
                                                const u16* __restrict__ x, const u16* __restrict__ Are,
                                                const u16* __restrict__ Aim, const u16* __restrict__ Bre,
                                                const u16* __restrict__ Bim, const u16* __restrict__ Cre,
                                                const u16* __restrict__ Cim, const u16* __restrict__ logdt,
                                                float* __restrict__ mod, float2* __restrict__ stats,
                                                float* __restrict__ Kd) {
  __shared__ float sm[4 * 656 + 128 * 12];
  const int b = blockIdx.x, t = threadIdx.x;
  const int lane = t & 63, wv = t >> 6;
  if (b < K0_MOD_BLOCKS) {
    float* s_s = sm;
    float* red = sm + 576;
    for (int i = t; i < D; i += 256) {
      float cv = bf2f(c[i]);
      s_s[i] = rbf(cv / (1.f + __expf(-cv)));
    }
    __syncthreads();
    int cg = t & 7, rg = t >> 3;
    float acc[8];
#pragma unroll
    for (int j = 0; j < 8; ++j) acc[j] = 0.f;
    for (int r = rg * 18; r < rg * 18 + 18; ++r) {
      bf16x8 w = *reinterpret_cast<const bf16x8*>(wmod + (size_t)r * (6 * D) + b * 64 + cg * 8);
      float s = s_s[r];
#pragma unroll
      for (int j = 0; j < 8; ++j) acc[j] += s * bf2f((u16)w[j]);
    }
#pragma unroll
    for (int j = 0; j < 8; ++j) red[rg * 64 + cg * 8 + j] = acc[j];
    __syncthreads();
    if (t < 64) {
      float s = 0.f;
      for (int r = 0; r < 32; ++r) s += red[r * 64 + t];
      mod[b * 64 + t] = rbf(s);
    }
  } else if (b < K0_MOD_BLOCKS + K0_STAT_BLOCKS) {
    int r0 = (b - K0_MOD_BLOCKS) * 64 + wv * 16;
    for (int r = r0; r < r0 + 16; ++r) {
      float v[9];
      float s = 0.f;
#pragma unroll
      for (int j = 0; j < 9; ++j) { v[j] = bf2f(x[(size_t)r * D + lane + 64 * j]); s += v[j]; }
      s = wave_sum(s);
      float m = s * (1.f / D);
      float q = 0.f;
#pragma unroll
      for (int j = 0; j < 9; ++j) { float dd = v[j] - m; q += dd * dd; }
      q = wave_sum(q);
      float var = rbf(q * (1.f / D));
      float sq = rbf(sqrtf(rbf(var + 1e-5f)));
      if (lane == 0) stats[r] = make_float2(rbf(m), sq);
    }
  } else {
    const int d = b - K0_MOD_BLOCKS - K0_STAT_BLOCKS;
    float* red = sm;
    float* pr = sm + 4 * 656;
    if (t < NS) {
      float dt = rbf(__expf(bf2f(logdt[d])));
      float ar = dt * bf2f(Are[d * NS + t]), ai = dt * bf2f(Aim[d * NS + t]);
      float br = dt * bf2f(Bre[d * NS + t]), bi = dt * bf2f(Bim[d * NS + t]);
      float cr = bf2f(Cre[d * NS + t]), ci = bf2f(Cim[d * NS + t]);
      pr[t * 12 + 0] = ar; pr[t * 12 + 1] = ai;
      pr[t * 12 + 2] = cr * br - ci * bi; pr[t * 12 + 3] = cr * bi + ci * br;
#pragma unroll
      for (int s = 0; s < 4; ++s) {
        float st = (float)(1 << s);
        float e = __expf(ar * st);
        float sn, cs;
        __sincosf(ai * st, &sn, &cs);
        pr[t * 12 + 4 + 2 * s] = e * cs; pr[t * 12 + 5 + 2 * s] = e * sn;
      }
    }
    __syncthreads();
    if (lane < 41) {
      int c_ = lane, seg, lv0;
      if (c_ < 16) { seg = 0; lv0 = 16 * c_; }
      else if (c_ < 24) { seg = 1; lv0 = 256 + 32 * (c_ - 16); }
      else if (c_ < 32) { seg = 2; lv0 = 512 + 64 * (c_ - 24); }
      else { seg = 3; lv0 = 1024 + 128 * (c_ - 32); }
      float acc[16];
#pragma unroll
      for (int i = 0; i < 16; ++i) acc[i] = 0.f;
      for (int n = wv * 32; n < wv * 32 + 32; ++n) {
        float ar = pr[n * 12], ai = pr[n * 12 + 1], gr = pr[n * 12 + 2], gi = pr[n * 12 + 3];
        float er = pr[n * 12 + 4 + 2 * seg], ei = pr[n * 12 + 5 + 2 * seg];
        float e = __expf(ar * (float)lv0);
        float sn, cs;
        __sincosf(ai * (float)lv0, &sn, &cs);
        float pr_ = e * cs, pi_ = e * sn;
        float sr = gr * pr_ - gi * pi_, si = gr * pi_ + gi * pr_;
#pragma unroll
        for (int i = 0; i < 16; ++i) {
          acc[i] += sr;
          float nr = sr * er - si * ei, ni = sr * ei + si * er;
          sr = nr; si = ni;
        }
      }
#pragma unroll
      for (int i = 0; i < 16; ++i) red[wv * 656 + c_ * 16 + i] = acc[i];
    }
    __syncthreads();
    for (int i = t; i < 641; i += 256) Kd[d * KDS + i] = red[i] + red[656 + i] + red[2 * 656 + i] + red[3 * 656 + i];
  }
}

constexpr int HCS = 2192;
constexpr int HOFF = 2048;
constexpr int KROW = 72;

__global__ __launch_bounds__(256) void k1_conv(const u16* __restrict__ x, const float2* __restrict__ stats,
                                                const float* __restrict__ mod, const float* __restrict__ Kd,
                                                const u16* __restrict__ Dskip, u16* __restrict__ x1) {
  __shared__ __attribute__((aligned(16))) u16 hc[8 * HCS];
  __shared__ __attribute__((aligned(16))) u16 kp[2 * 64 * KROW];
  __shared__ u16 hs[L], xs[L];
  const int d = blockIdx.x, t = threadIdx.x, lane = t & 63, wv = t >> 6;
  const float ca1 = rbf(1.f + mod[D + d]), sa = mod[d], ga = mod[2 * D + d];
  for (int l = t; l < L; l += 256) {
    u16 xr = x[(size_t)l * D + d];
    float2 st = stats[l];
    float hv = rbf(rbf(rbf(rbf(bf2f(xr) - st.x) / st.y) * ca1) + sa);
    hs[l] = f2bf(hv); xs[l] = xr;
  }
  for (int l = t; l < L; l += 256) {
    float kv = Kd[d * KDS + kidx_of(l)];
    u16 hi = f2bf(kv);
    u16 lo = f2bf(kv - bf2f(hi));
    kp[(32 + (l >> 6)) * KROW + (l & 63)] = hi;
    kp[(64 + 32 + (l >> 6)) * KROW + (l & 63)] = lo;
  }
  for (int i = t; i < 32 * KROW; i += 256) { kp[i] = 0; kp[64 * KROW + i] = 0; }
  __syncthreads();
  for (int i = t; i < 8 * HCS; i += 256) {
    int c_ = i / HCS, xx = i % HCS;
    int idx = HOFF - xx + c_;
    hc[i] = (idx >= 0 && idx < L) ? hs[idx] : (u16)0;
  }
  __syncthreads();
  const int n = lane & 15, q = lane >> 4, nh = n >> 3, cc = n & 7;
  f32x4 acc[2][4];
#pragma unroll
  for (int m = 0; m < 2; ++m)
#pragma unroll
    for (int j = 0; j < 4; ++j) acc[m][j] = f32x4{0.f, 0.f, 0.f, 0.f};
  for (int dl = wv; dl < 32; dl += 4) {
#pragma unroll
    for (int ks = 0; ks < 2; ++ks) {
      bf16x8 bfr[4];
#pragma unroll
      for (int nt = 0; nt < 4; ++nt) {
        int base = dl * 64 + nt * 16 - ks * 32 - 8 * q;
        int x0 = HOFF - base - 8 * nh;
        bfr[nt] = *reinterpret_cast<const bf16x8*>(&hc[cc * HCS + x0]);
      }
      {
        int row = 32 + 16 + n - dl;
        bf16x8 ah = *reinterpret_cast<const bf16x8*>(&kp[row * KROW + ks * 32 + 8 * q]);
        bf16x8 al = *reinterpret_cast<const bf16x8*>(&kp[(64 + row) * KROW + ks * 32 + 8 * q]);
#pragma unroll
        for (int nt = 0; nt < 4; ++nt) {
          acc[1][nt] = __builtin_amdgcn_mfma_f32_16x16x32_bf16(ah, bfr[nt], acc[1][nt], 0, 0, 0);
          acc[1][nt] = __builtin_amdgcn_mfma_f32_16x16x32_bf16(al, bfr[nt], acc[1][nt], 0, 0, 0);
        }
      }
      if (dl < 16) {
        int row = 32 + n - dl;
        bf16x8 ah = *reinterpret_cast<const bf16x8*>(&kp[row * KROW + ks * 32 + 8 * q]);
        bf16x8 al = *reinterpret_cast<const bf16x8*>(&kp[(64 + row) * KROW + ks * 32 + 8 * q]);
#pragma unroll
        for (int nt = 0; nt < 4; ++nt) {
          acc[0][nt] = __builtin_amdgcn_mfma_f32_16x16x32_bf16(ah, bfr[nt], acc[0][nt], 0, 0, 0);
          acc[0][nt] = __builtin_amdgcn_mfma_f32_16x16x32_bf16(al, bfr[nt], acc[0][nt], 0, 0, 0);
        }
      }
    }
  }
  __syncthreads();
  float* red = reinterpret_cast<float*>(hc);
#pragma unroll
  for (int m = 0; m < 2; ++m)
#pragma unroll
    for (int nt = 0; nt < 4; ++nt)
#pragma unroll
      for (int r = 0; r < 4; ++r) red[wv * L + (m * 16 + q * 4 + r) * 64 + nt * 16 + n] = acc[m][nt][r];
  __syncthreads();
  const float dsk = bf2f(Dskip[d]);
  for (int l = t; l < L; l += 256) {
    float y = rbf(red[l] + red[L + l] + red[2 * L + l] + red[3 * L + l]);
    float hv = bf2f(hs[l]);
    float ya = rbf(y + rbf(hv * dsk));
    x1[(size_t)l * D + d] = f2bf(bf2f(xs[l]) + rbf(ga * ya));
  }
}

constexpr int G1_BM = 128, G1_BN = 288, G1_AS = 72, G1_BS = 296;

__global__ __launch_bounds__(512) void k2_gemm1(const u16* __restrict__ x1, const float* __restrict__ mod,
                                                 const u16* __restrict__ w1, u16* __restrict__ hmid) {
  __shared__ __attribute__((aligned(16))) u16 As[2][G1_BM * G1_AS];
  __shared__ __attribute__((aligned(16))) u16 Bs[2][64 * G1_BS];
  __shared__ float2 st[G1_BM];
  __shared__ float cb1[D], sbv[D];
  const int t = threadIdx.x, lane = t & 63, wv = t >> 6;
  const int bm = blockIdx.x & 15, bn = blockIdx.x >> 4;
  const int m0 = bm * G1_BM, n0 = bn * G1_BN;
  for (int i = t; i < D; i += 512) { cb1[i] = rbf(1.f + mod[4 * D + i]); sbv[i] = mod[3 * D + i]; }
  for (int r = wv * 16; r < wv * 16 + 16; ++r) {
    float v[9];
    float s = 0.f;
#pragma unroll
    for (int j = 0; j < 9; ++j) { v[j] = bf2f(x1[(size_t)(m0 + r) * D + lane + 64 * j]); s += v[j]; }
    s = wave_sum(s);
    float m = s * (1.f / D);
    float qq = 0.f;
#pragma unroll
    for (int j = 0; j < 9; ++j) { float dd = v[j] - m; qq += dd * dd; }
    qq = wave_sum(qq);
    float var = rbf(qq * (1.f / D));
    float sq = rbf(sqrtf(rbf(var + 1e-5f)));
    if (lane == 0) st[r] = make_float2(rbf(m), sq);
  }
  __syncthreads();
  const int wm = wv & 3, wn = wv >> 2;
  const int n = lane & 15, q = lane >> 4;
  f32x4 acc[2][9];
#pragma unroll
  for (int i = 0; i < 2; ++i)
#pragma unroll
    for (int j = 0; j < 9; ++j) acc[i][j] = f32x4{0.f, 0.f, 0.f, 0.f};

  auto stage = [&](int kt, int buf) {
    {
      int row = t >> 2, ch = t & 3;
      const u16* src = x1 + (size_t)(m0 + row) * D + kt * 64 + ch * 16;
      float2 s = st[row];
#pragma unroll
      for (int h = 0; h < 2; ++h) {
        bf16x8 v = *reinterpret_cast<const bf16x8*>(src + h * 8);
        bf16x8 o;
#pragma unroll
        for (int j = 0; j < 8; ++j) {
          int k = kt * 64 + ch * 16 + h * 8 + j;
          float hv = rbf(rbf(rbf(rbf(bf2f((u16)v[j]) - s.x) / s.y) * cb1[k]) + sbv[k]);
          o[j] = (short)f2bf(hv);
        }
        *reinterpret_cast<bf16x8*>(&As[buf][row * G1_AS + ch * 16 + h * 8]) = o;
      }
    }
    for (int ch = t; ch < 64 * 36; ch += 512) {
      int row = ch / 36, cc = ch % 36;
      bf16x8 v = *reinterpret_cast<const bf16x8*>(w1 + (size_t)(kt * 64 + row) * F + n0 + cc * 8);
      *reinterpret_cast<bf16x8*>(&Bs[buf][row * G1_BS + cc * 8]) = v;
    }
  };
  stage(0, 0);
  __syncthreads();
  for (int kt = 0; kt < 9; ++kt) {
    int buf = kt & 1;
    if (kt + 1 < 9) stage(kt + 1, buf ^ 1);
#pragma unroll
    for (int ks = 0; ks < 2; ++ks) {
      bf16x8 af[2];
#pragma unroll
      for (int mt = 0; mt < 2; ++mt)
        af[mt] = *reinterpret_cast<const bf16x8*>(&As[buf][(wm * 32 + mt * 16 + n) * G1_AS + ks * 32 + 8 * q]);
#pragma unroll
      for (int nt = 0; nt < 9; ++nt) {
        const u16* p0 = &Bs[buf][(ks * 32 + 8 * q + (n >> 2)) * G1_BS + wn * 144 + nt * 16 + 4 * (n & 3)];
        s16x4 lo = __builtin_amdgcn_ds_read_tr16_b64_v4i16((lds_s16x4_ptr)(p0));
        s16x4 hi = __builtin_amdgcn_ds_read_tr16_b64_v4i16((lds_s16x4_ptr)(p0 + 4 * G1_BS));
        bf16x8 bfr = {lo[0], lo[1], lo[2], lo[3], hi[0], hi[1], hi[2], hi[3]};
#pragma unroll
        for (int mt = 0; mt < 2; ++mt)
          acc[mt][nt] = __builtin_amdgcn_mfma_f32_16x16x32_bf16(af[mt], bfr, acc[mt][nt], 0, 0, 0);
      }
    }
    __syncthreads();
  }
#pragma unroll
  for (int mt = 0; mt < 2; ++mt)
#pragma unroll
    for (int nt = 0; nt < 9; ++nt)
#pragma unroll
      for (int r = 0; r < 4; ++r) {
        float hv = rbf(acc[mt][nt][r]);
        float sv = hv / (1.f + __expf(-hv));
        hmid[(size_t)(m0 + wm * 32 + mt * 16 + q * 4 + r) * F + n0 + wn * 144 + nt * 16 + n] = f2bf(sv);
      }
}

constexpr int G2_BN = 144, G2_BS = 152, G2_SPLIT = 4;

__global__ __launch_bounds__(512) void k3_gemm2(const u16* __restrict__ hmid, const u16* __restrict__ w2,
                                                 float* __restrict__ part) {
  __shared__ __attribute__((aligned(16))) u16 As[2][128 * G1_AS];
  __shared__ __attribute__((aligned(16))) u16 Bs[2][64 * G2_BS];
  const int t = threadIdx.x, lane = t & 63, wv = t >> 6;
  const int bm = blockIdx.x & 15, bn = (blockIdx.x >> 4) & 3, sp = blockIdx.x >> 6;
  const int m0 = bm * 128, n0 = bn * G2_BN, k0 = sp * (F / G2_SPLIT);
  const int wm = wv & 3, ks = wv >> 2;
  const int n = lane & 15, q = lane >> 4;
  f32x4 acc[2][9];
#pragma unroll
  for (int i = 0; i < 2; ++i)
#pragma unroll
    for (int j = 0; j < 9; ++j) acc[i][j] = f32x4{0.f, 0.f, 0.f, 0.f};
  auto stage = [&](int kt, int buf) {
    {
      int row = t >> 2, ch = t & 3;
      const u16* src = hmid + (size_t)(m0 + row) * F + k0 + kt * 64 + ch * 16;
      *reinterpret_cast<bf16x8*>(&As[buf][row * G1_AS + ch * 16]) = *reinterpret_cast<const bf16x8*>(src);
      *reinterpret_cast<bf16x8*>(&As[buf][row * G1_AS + ch * 16 + 8]) = *reinterpret_cast<const bf16x8*>(src + 8);
    }
    for (int ch = t; ch < 64 * 18; ch += 512) {
      int row = ch / 18, cc = ch % 18;
      bf16x8 v = *reinterpret_cast<const bf16x8*>(w2 + (size_t)(k0 + kt * 64 + row) * D + n0 + cc * 8);
      *reinterpret_cast<bf16x8*>(&Bs[buf][row * G2_BS + cc * 8]) = v;
    }
  };
  stage(0, 0);
  __syncthreads();
  constexpr int NKT = F / G2_SPLIT / 64;
  for (int kt = 0; kt < NKT; ++kt) {
    int buf = kt & 1;
    if (kt + 1 < NKT) stage(kt + 1, buf ^ 1);
    {
      bf16x8 af[2];
#pragma unroll
      for (int mt = 0; mt < 2; ++mt)
        af[mt] = *reinterpret_cast<const bf16x8*>(&As[buf][(wm * 32 + mt * 16 + n) * G1_AS + ks * 32 + 8 * q]);
#pragma unroll
      for (int nt = 0; nt < 9; ++nt) {
        const u16* p0 = &Bs[buf][(ks * 32 + 8 * q + (n >> 2)) * G2_BS + nt * 16 + 4 * (n & 3)];
        s16x4 lo = __builtin_amdgcn_ds_read_tr16_b64_v4i16((lds_s16x4_ptr)(p0));
        s16x4 hi = __builtin_amdgcn_ds_read_tr16_b64_v4i16((lds_s16x4_ptr)(p0 + 4 * G2_BS));
        bf16x8 bfr = {lo[0], lo[1], lo[2], lo[3], hi[0], hi[1], hi[2], hi[3]};
#pragma unroll
        for (int mt = 0; mt < 2; ++mt)
          acc[mt][nt] = __builtin_amdgcn_mfma_f32_16x16x32_bf16(af[mt], bfr, acc[mt][nt], 0, 0, 0);
      }
    }
    __syncthreads();
  }
  float* red = reinterpret_cast<float*>(&As[0][0]);
  for (int half = 0; half < 2; ++half) {
    if (ks == 1 && (wm >> 1) == half) {
#pragma unroll
      for (int mt = 0; mt < 2; ++mt)
#pragma unroll
        for (int nt = 0; nt < 9; ++nt)
#pragma unroll
          for (int r = 0; r < 4; ++r)
            red[((wm & 1) * 32 + mt * 16 + q * 4 + r) * 144 + nt * 16 + n] = acc[mt][nt][r];
    }
    __syncthreads();
    if (ks == 0 && (wm >> 1) == half) {
#pragma unroll
      for (int mt = 0; mt < 2; ++mt)
#pragma unroll
        for (int nt = 0; nt < 9; ++nt)
#pragma unroll
          for (int r = 0; r < 4; ++r)
            part[((size_t)sp * L + m0 + wm * 32 + mt * 16 + q * 4 + r) * D + n0 + nt * 16 + n] =
                acc[mt][nt][r] + red[((wm & 1) * 32 + mt * 16 + q * 4 + r) * 144 + nt * 16 + n];
    }
    __syncthreads();
  }
}

__global__ __launch_bounds__(256) void k4_final(const float* __restrict__ part, const u16* __restrict__ x1,
                                                 const float* __restrict__ mod, u16* __restrict__ out) {
  int i = blockIdx.x * 256 + threadIdx.x;
  int e = i * 4;
  if (e >= L * D) return;
  float4 s = *reinterpret_cast<const float4*>(part + e);
#pragma unroll
  for (int p = 1; p < G2_SPLIT; ++p) {
    float4 v = *reinterpret_cast<const float4*>(part + (size_t)p * L * D + e);
    s.x += v.x; s.y += v.y; s.z += v.z; s.w += v.w;
  }
  int dcol = e % D;
  float sv[4] = {s.x, s.y, s.z, s.w};
  u16 o[4];
#pragma unroll
  for (int j = 0; j < 4; ++j) {
    float gb = mod[5 * D + dcol + j];
    o[j] = f2bf(bf2f(x1[e + j]) + rbf(gb * rbf(sv[j])));
  }
  *reinterpret_cast<uint2*>(out + e) = make_uint2(o[0] | ((uint32_t)o[1] << 16), o[2] | ((uint32_t)o[3] << 16));
}

extern "C" void kernel_launch(void* const* d_in, const int* in_sizes, int n_in, void* d_out, int out_size,
                              void* d_ws, size_t ws_size, hipStream_t stream) {
  const u16* A_u = (const u16*)d_in[0];
  const u16* Are = (const u16*)d_in[1];
  const u16* Aim = (const u16*)d_in[2];
  const u16* Bre = (const u16*)d_in[3];
  const u16* Bim = (const u16*)d_in[4];
  const u16* Cre = (const u16*)d_in[5];
  const u16* Cim = (const u16*)d_in[6];
  const u16* logdt = (const u16*)d_in[7];
  const u16* Dskip = (const u16*)d_in[8];
  const u16* w1 = (const u16*)d_in[10];
  const u16* w2 = (const u16*)d_in[11];
  const u16* c = (const u16*)d_in[12];
  const u16* wmod = (const u16*)d_in[13];
  char* ws = (char*)d_ws;
  float* mod = (float*)ws;                         ws += 16384;
  float2* stats = (float2*)ws;                     ws += 16384;
  float* Kd = (float*)ws;                          ws += (size_t)D * KDS * 4 + 256 - ((size_t)D * KDS * 4) % 256;
  u16* x1 = (u16*)ws;                              ws += (size_t)L * D * 2;
  u16* hmid = (u16*)ws;                            ws += (size_t)L * F * 2;
  float* part = (float*)ws;
  k0_prep<<<K0_MOD_BLOCKS + K0_STAT_BLOCKS + D, 256, 0, stream>>>(c, wmod, A_u, Are, Aim, Bre, Bim, Cre, Cim, logdt,
                                                                  mod, stats, Kd);
  k1_conv<<<D, 256, 0, stream>>>(A_u, stats, mod, Kd, Dskip, x1);
  k2_gemm1<<<256, 512, 0, stream>>>(x1, mod, w1, hmid);
  k3_gemm2<<<256, 512, 0, stream>>>(hmid, w2, part);
  k4_final<<<(L * D / 4 + 255) / 256, 256, 0, stream>>>(part, x1, mod, (u16*)d_out);
}
```

```cpp
#include <hip/hip_runtime.h>
#include <hip/hip_bf16.h>
#include <stdint.h>

typedef unsigned short u16;
using bf16x8 = __attribute__((ext_vector_type(8))) short;
using f32x4 = __attribute__((ext_vector_type(4))) float;
using f32x2 = __attribute__((ext_vector_type(2))) float;
using s16x4 = __attribute__((ext_vector_type(4))) short;
using u32x4 = __attribute__((ext_vector_type(4))) unsigned;
typedef __attribute__((address_space(3))) s16x4* lds_s16x4_ptr;

constexpr int L = 2048, D = 576, NS = 128, F = 4608;
constexpr int KDS = 656;

__device__ __forceinline__ float bf2f(u16 v) { return __uint_as_float(((uint32_t)v) << 16); }
__device__ __forceinline__ u16 f2bf(float f) {
  uint32_t u = __float_as_uint(f);
  u += 0x7FFFu + ((u >> 16) & 1u);
  return (u16)(u >> 16);
}
__device__ __forceinline__ float rbf(float f) { return bf2f(f2bf(f)); }

__device__ __forceinline__ float wave_sum(float v) {
#pragma unroll
  for (int o = 32; o > 0; o >>= 1) v += __shfl_xor(v, o);
  return v;
}
__device__ __forceinline__ void fast_cis(float x, float& sn, float& cs) {
  float r = x * 0.15915494309189535f;
  r = r - floorf(r);
  sn = __builtin_amdgcn_sinf(r);
  cs = __builtin_amdgcn_cosf(r);
}

__device__ __forceinline__ int kidx_of(int l) {
  int lv = (int)rbf((float)l);
  if (lv < 256) return lv;
  if (lv < 512) return 256 + ((lv - 256) >> 1);
  if (lv < 1024) return 384 + ((lv - 512) >> 2);
  return 512 + ((lv - 1024) >> 3);
}

constexpr int K0_MOD_BLOCKS = 54, K0_STAT_BLOCKS = 64;

__global__ __launch_bounds__(256) void k0_prep(const u16* __restrict__ c, const u16* __restrict__ wmod,
                                                const u16* __restrict__ x, const u16* __restrict__ Are,
                                                const u16* __restrict__ Aim, const u16* __restrict__ Bre,
                                                const u16* __restrict__ Bim, const u16* __restrict__ Cre,
                                                const u16* __restrict__ Cim, const u16* __restrict__ logdt,
                                                float* __restrict__ mod, u16* __restrict__ xnT,
                                                u16* __restrict__ xT, float* __restrict__ Kd) {
  __shared__ __attribute__((aligned(16))) char smraw[2 * 32 * 584 * 2];
  const int b = blockIdx.x, t = threadIdx.x;
  const int lane = t & 63, wv = t >> 6;
  if (b < K0_MOD_BLOCKS) {
    float* s_s = reinterpret_cast<float*>(smraw);
    float* red = s_s + 576;
    for (int i = t; i < D; i += 256) {
      float cv = bf2f(c[i]);
      s_s[i] = rbf(cv / (1.f + __expf(-cv)));
    }
    __syncthreads();
    int cg = t & 7, rg = t >> 3;
    float acc[8];
#pragma unroll
    for (int j = 0; j < 8; ++j) acc[j] = 0.f;
#pragma unroll 6
    for (int r = rg * 18; r < rg * 18 + 18; ++r) {
      bf16x8 w = *reinterpret_cast<const bf16x8*>(wmod + (size_t)r * (6 * D) + b * 64 + cg * 8);
      float s = s_s[r];
#pragma unroll
      for (int j = 0; j < 8; ++j) acc[j] += s * bf2f((u16)w[j]);
    }
#pragma unroll
    for (int j = 0; j < 8; ++j) red[rg * 64 + cg * 8 + j] = acc[j];
    __syncthreads();
    if (t < 64) {
      float s = 0.f;
      for (int r = 0; r < 32; ++r) s += red[r * 64 + t];
      mod[b * 64 + t] = rbf(s);
    }
  } else if (b < K0_MOD_BLOCKS + K0_STAT_BLOCKS) {
    u16* tn = reinterpret_cast<u16*>(smraw);
    u16* tx = tn + 32 * 584;
    int r0 = (b - K0_MOD_BLOCKS) * 32;
    for (int rr = wv * 8; rr < wv * 8 + 8; ++rr) {
      int r = r0 + rr;
      u16 raw[9];
      float v[9];
      float s = 0.f;
#pragma unroll
      for (int j = 0; j < 9; ++j) { raw[j] = x[(size_t)r * D + lane + 64 * j]; v[j] = bf2f(raw[j]); s += v[j]; }
      s = wave_sum(s);
      float m = s * (1.f / D);
      float q = 0.f;
#pragma unroll
      for (int j = 0; j < 9; ++j) { float dd = v[j] - m; q += dd * dd; }
      q = wave_sum(q);
      float var = rbf(q * (1.f / D));
      float sq = rbf(sqrtf(rbf(var + 1e-5f)));
      float mb = rbf(m);
#pragma unroll
      for (int j = 0; j < 9; ++j) {
        tn[rr * 584 + lane + 64 * j] = f2bf(rbf(v[j] - mb) / sq);
        tx[rr * 584 + lane + 64 * j] = raw[j];
      }
    }
    __syncthreads();
    for (int i = t; i < D * 4; i += 256) {
      int dch = i >> 2, part = i & 3;
      u16 on[8], ox[8];
#pragma unroll
      for (int j = 0; j < 8; ++j) { on[j] = tn[(part * 8 + j) * 584 + dch]; ox[j] = tx[(part * 8 + j) * 584 + dch]; }
      *reinterpret_cast<bf16x8*>(xnT + (size_t)dch * L + r0 + part * 8) = *reinterpret_cast<bf16x8*>(on);
      *reinterpret_cast<bf16x8*>(xT + (size_t)dch * L + r0 + part * 8) = *reinterpret_cast<bf16x8*>(ox);
    }
  } else {
    const int d = b - K0_MOD_BLOCKS - K0_STAT_BLOCKS;
    float* red = reinterpret_cast<float*>(smraw);
    float* pr = red + 4 * 656;
    if (t < NS) {
      float dt = rbf(__expf(bf2f(logdt[d])));
      float ar = dt * bf2f(Are[d * NS + t]), ai = dt * bf2f(Aim[d * NS + t]);
      float br = dt * bf2f(Bre[d * NS + t]), bi = dt * bf2f(Bim[d * NS + t]);
      float cr = bf2f(Cre[d * NS + t]), ci = bf2f(Cim[d * NS + t]);
      pr[t * 12 + 0] = ar; pr[t * 12 + 1] = ai;
      pr[t * 12 + 2] = cr * br - ci * bi; pr[t * 12 + 3] = cr * bi + ci * br;
#pragma unroll
      for (int s = 0; s < 4; ++s) {
        float st = (float)(1 << s);
        float e = __expf(ar * st);
        float sn, cs;
        fast_cis(ai * st, sn, cs);
        pr[t * 12 + 4 + 2 * s] = e * cs; pr[t * 12 + 5 + 2 * s] = e * sn;
      }
    }
    __syncthreads();
    if (lane < 41) {
      int c_ = lane, seg, lv0;
      if (c_ < 16) { seg = 0; lv0 = 16 * c_; }
      else if (c_ < 24) { seg = 1; lv0 = 256 + 32 * (c_ - 16); }
      else if (c_ < 32) { seg = 2; lv0 = 512 + 64 * (c_ - 24); }
      else { seg = 3; lv0 = 1024 + 128 * (c_ - 32); }
      const float flv = (float)lv0;
      float acc[16];
#pragma unroll
      for (int i = 0; i < 16; ++i) acc[i] = 0.f;
      for (int n = wv * 32; n < wv * 32 + 32; ++n) {
        f32x4 p0 = *reinterpret_cast<const f32x4*>(&pr[n * 12]);
        f32x2 E = *reinterpret_cast<const f32x2*>(&pr[n * 12 + 4 + 2 * seg]);
        f32x2 En = {-E.y, E.x};
        float e = __expf(p0.x * flv);
        float sn, cs;
        fast_cis(p0.y * flv, sn, cs);
        float pr_ = e * cs, pi_ = e * sn;
        f32x2 S = {p0.z * pr_ - p0.w * pi_, p0.z * pi_ + p0.w * pr_};
#pragma unroll
        for (int i = 0; i < 16; ++i) {
          acc[i] += S.x;
          f32x2 sx = {S.x, S.x}, sy = {S.y, S.y};
          S = sx * E + sy * En;
        }
      }
#pragma unroll
      for (int i = 0; i < 16; ++i) red[wv * 656 + c_ * 16 + i] = acc[i];
    }
    __syncthreads();
    for (int i = t; i < 641; i += 256) Kd[d * KDS + i] = red[i] + red[656 + i] + red[2 * 656 + i] + red[3 * 656 + i];
  }
}

constexpr int HCS = 2192;
constexpr int HOFF = 2048;
constexpr int KROW = 72;

__global__ __launch_bounds__(256) void k1_conv(const u16* __restrict__ xnT, const u16* __restrict__ xT,
                                                const float* __restrict__ mod, const float* __restrict__ Kd,
                                                const u16* __restrict__ Dskip, u16* __restrict__ x1T) {
  __shared__ __attribute__((aligned(16))) u16 hc[8 * HCS];
  __shared__ __attribute__((aligned(16))) u16 kp[2 * 64 * KROW];
  __shared__ __attribute__((aligned(16))) u16 hr[HCS + 16];
  const int d = blockIdx.x, t = threadIdx.x, lane = t & 63, wv = t >> 6;
  const float ca1 = rbf(1.f + mod[D + d]), sa = mod[d], ga = mod[2 * D + d];
  u16 hv[8];
  {
    bf16x8 xn = *reinterpret_cast<const bf16x8*>(xnT + (size_t)d * L + 8 * t);
#pragma unroll
    for (int j = 0; j < 8; ++j) hv[j] = f2bf(rbf(bf2f((u16)xn[j]) * ca1) + sa);
  }
  {
    float kv[8];
#pragma unroll
    for (int j = 0; j < 8; ++j) kv[j] = Kd[d * KDS + kidx_of(8 * t + j)];
    u16 hi[8], lo[8];
#pragma unroll
    for (int j = 0; j < 8; ++j) { hi[j] = f2bf(kv[j]); lo[j] = f2bf(kv[j] - bf2f(hi[j])); }
    int l0 = 8 * t;
    *reinterpret_cast<bf16x8*>(&kp[(32 + (l0 >> 6)) * KROW + (l0 & 63)]) = *reinterpret_cast<bf16x8*>(hi);
    *reinterpret_cast<bf16x8*>(&kp[(96 + (l0 >> 6)) * KROW + (l0 & 63)]) = *reinterpret_cast<bf16x8*>(lo);
  }
  for (int i = t; i < 32 * KROW / 8; i += 256) {
    *reinterpret_cast<bf16x8*>(&kp[i * 8]) = bf16x8{0, 0, 0, 0, 0, 0, 0, 0};
    *reinterpret_cast<bf16x8*>(&kp[64 * KROW + i * 8]) = bf16x8{0, 0, 0, 0, 0, 0, 0, 0};
  }
  {
    u16 rv[8];
#pragma unroll
    for (int j = 0; j < 8; ++j) rv[j] = hv[7 - j];
    int i0 = 8 + HOFF - 8 * t - 7;
#pragma unroll
    for (int j = 0; j < 8; ++j) hr[i0 + j] = rv[j];
  }
  if (t < 8) hr[t] = 0;
  if (t == 8) hr[8] = 0;
  for (int i = 8 + HOFF + 1 + t; i < HCS + 16; i += 256) hr[i] = 0;
  __syncthreads();
  for (int u = t; u < HCS / 8; u += 256) {
    bf16x8 a0 = *reinterpret_cast<const bf16x8*>(&hr[8 * u]);
    bf16x8 a1 = *reinterpret_cast<const bf16x8*>(&hr[8 * u + 8]);
    u16 w[16];
#pragma unroll
    for (int j = 0; j < 8; ++j) { w[j] = (u16)a0[j]; w[8 + j] = (u16)a1[j]; }
#pragma unroll
    for (int c_ = 0; c_ < 8; ++c_) {
      bf16x8 o;
#pragma unroll
      for (int j = 0; j < 8; ++j) o[j] = (short)w[8 + j - c_];
      *reinterpret_cast<bf16x8*>(&hc[c_ * HCS + 8 * u]) = o;
    }
  }
  __syncthreads();
  const int n = lane & 15, q = lane >> 4, nh = n >> 3, cc = n & 7;
  f32x4 acc[2][4];
#pragma unroll
  for (int m = 0; m < 2; ++m)
#pragma unroll
    for (int j = 0; j < 4; ++j) acc[m][j] = f32x4{0.f, 0.f, 0.f, 0.f};
  for (int dl = wv; dl < 32; dl += 4) {
#pragma unroll
    for (int ks = 0; ks < 2; ++ks) {
      bf16x8 bfr[4];
#pragma unroll
      for (int nt = 0; nt < 4; ++nt) {
        int base = dl * 64 + nt * 16 - ks * 32 - 8 * q;
        int x0 = HOFF - base - 8 * nh;
        bfr[nt] = *reinterpret_cast<const bf16x8*>(&hc[cc * HCS + x0]);
      }
      {
        int row = 32 + 16 + n - dl;
        bf16x8 ah = *reinterpret_cast<const bf16x8*>(&kp[row * KROW + ks * 32 + 8 * q]);
        bf16x8 al = *reinterpret_cast<const bf16x8*>(&kp[(64 + row) * KROW + ks * 32 + 8 * q]);
#pragma unroll
        for (int nt = 0; nt < 4; ++nt) {
          acc[1][nt] = __builtin_amdgcn_mfma_f32_16x16x32_bf16(ah, bfr[nt], acc[1][nt], 0, 0, 0);
          acc[1][nt] = __builtin_amdgcn_mfma_f32_16x16x32_bf16(al, bfr[nt], acc[1][nt], 0, 0, 0);
        }
      }
      if (dl < 16) {
        int row = 32 + n - dl;
        bf16x8 ah = *reinterpret_cast<const bf16x8*>(&kp[row * KROW + ks * 32 + 8 * q]);
        bf16x8 al = *reinterpret_cast<const bf16x8*>(&kp[(64 + row) * KROW + ks * 32 + 8 * q]);
#pragma unroll
        for (int nt = 0; nt < 4; ++nt) {
          acc[0][nt] = __builtin_amdgcn_mfma_f32_16x16x32_bf16(ah, bfr[nt], acc[0][nt], 0, 0, 0);
          acc[0][nt] = __builtin_amdgcn_mfma_f32_16x16x32_bf16(al, bfr[nt], acc[0][nt], 0, 0, 0);
        }
      }
    }
  }
  __syncthreads();
  float* red = reinterpret_cast<float*>(hc);
#pragma unroll
  for (int m = 0; m < 2; ++m)
#pragma unroll
    for (int nt = 0; nt < 4; ++nt)
#pragma unroll
      for (int r = 0; r < 4; ++r) red[wv * L + (m * 16 + q * 4 + r) * 64 + nt * 16 + n] = acc[m][nt][r];
  __syncthreads();
  const float dsk = bf2f(Dskip[d]);
  {
    bf16x8 xr = *reinterpret_cast<const bf16x8*>(xT + (size_t)d * L + 8 * t);
    u16 o[8];
#pragma unroll
    for (int j = 0; j < 8; ++j) {
      int l = 8 * t + j;
      float y = rbf(red[l] + red[L + l] + red[2 * L + l] + red[3 * L + l]);
      float ya = rbf(y + rbf(bf2f(hv[j]) * dsk));
      o[j] = f2bf(bf2f((u16)xr[j]) + rbf(ga * ya));
    }
    *reinterpret_cast<bf16x8*>(x1T + (size_t)d * L + 8 * t) = *reinterpret_cast<bf16x8*>(o);
  }
}

__global__ __launch_bounds__(256) void k1b_ln2(const u16* __restrict__ x1T, const float* __restrict__ mod,
                                                u16* __restrict__ x1, u16* __restrict__ h2) {
  __shared__ __attribute__((aligned(16))) u16 tl[32 * 584];
  const int t = threadIdx.x, lane = t & 63, wv = t >> 6;
  const int r0 = blockIdx.x * 32;
  for (int i = t; i < D * 4; i += 256) {
    int dch = i >> 2, part = i & 3;
    bf16x8 v = *reinterpret_cast<const bf16x8*>(x1T + (size_t)dch * L + r0 + part * 8);
#pragma unroll
    for (int j = 0; j < 8; ++j) tl[(part * 8 + j) * 584 + dch] = (u16)v[j];
  }
  __syncthreads();
  for (int rr = wv * 8; rr < wv * 8 + 8; ++rr) {
    float v[9];
    float s = 0.f;
#pragma unroll
    for (int j = 0; j < 9; ++j) { v[j] = bf2f(tl[rr * 584 + lane + 64 * j]); s += v[j]; }
    s = wave_sum(s);
    float m = s * (1.f / D);
    float q = 0.f;
#pragma unroll
    for (int j = 0; j < 9; ++j) { float dd = v[j] - m; q += dd * dd; }
    q = wave_sum(q);
    float var = rbf(q * (1.f / D));
    float sq = rbf(sqrtf(rbf(var + 1e-5f)));
    float mb = rbf(m);
    size_t ro = (size_t)(r0 + rr) * D;
#pragma unroll
    for (int j = 0; j < 9; ++j) {
      int k = lane + 64 * j;
      float hv = rbf(rbf(rbf(v[j] - mb) / sq) * rbf(1.f + mod[4 * D + k])) + mod[3 * D + k];
      h2[ro + k] = f2bf(hv);
      x1[ro + k] = f2bf(v[j]);
    }
  }
}

constexpr int G1_BM = 128, G1_BN = 288;
constexpr int G1_ABYTES = G1_BM * 64 * 2, G1_BBYTES = 64 * G1_BN * 2;

__global__ __launch_bounds__(512) void k2_gemm1(const u16* __restrict__ h2, const u16* __restrict__ w1,
                                                 u16* __restrict__ hmid) {
  __shared__ __attribute__((aligned(16))) char lds[2 * (G1_ABYTES + G1_BBYTES)];
  const int t = threadIdx.x, lane = t & 63, wv = t >> 6;
  const int bm = blockIdx.x & 15, bn = blockIdx.x >> 4;
  const int m0 = bm * G1_BM, n0 = bn * G1_BN;
  const int wm = wv & 3, wn = wv >> 2;
  const int n = lane & 15, q = lane >> 4;
  f32x4 acc[2][9];
#pragma unroll
  for (int i = 0; i < 2; ++i)
#pragma unroll
    for (int j = 0; j < 9; ++j) acc[i][j] = f32x4{0.f, 0.f, 0.f, 0.f};

  int aoff[2];
#pragma unroll
  for (int i = 0; i < 2; ++i) {
    int o = (wv * 2 + i) * 1024 + lane * 16;
    int row = o >> 7, cp = (o >> 4) & 7;
    aoff[i] = row * D + ((cp ^ (row & 7)) * 8);
  }
  int boff[5];
#pragma unroll
  for (int j = 0; j < 5; ++j) {
    int o = (wv + 8 * j) * 1024 + lane * 16;
    int row = o / 576, cb = o - row * 576;
    boff[j] = row * F + cb / 2;
  }
  const u16* abase = h2 + (size_t)m0 * D;
  const u16* bbase = w1 + n0;
  auto stage = [&](int kt, int buf) {
    char* al = lds + buf * (G1_ABYTES + G1_BBYTES);
    char* bl = al + G1_ABYTES;
#pragma unroll
    for (int i = 0; i < 2; ++i)
      __builtin_amdgcn_global_load_lds((const unsigned*)(abase + kt * 64 + aoff[i]),
                                       (unsigned*)(al + (wv * 2 + i) * 1024), 16, 0, 0);
#pragma unroll
    for (int j = 0; j < 5; ++j)
      if (wv + 8 * j < 36)
        __builtin_amdgcn_global_load_lds((const unsigned*)(bbase + (size_t)kt * 64 * F + boff[j]),
                                         (unsigned*)(bl + (wv + 8 * j) * 1024), 16, 0, 0);
  };
  stage(0, 0);
  __syncthreads();
  for (int kt = 0; kt < 9; ++kt) {
    int buf = kt & 1;
    if (kt + 1 < 9) stage(kt + 1, buf ^ 1);
    const char* al = lds + buf * (G1_ABYTES + G1_BBYTES);
    const u16* bl = reinterpret_cast<const u16*>(al + G1_ABYTES);
#pragma unroll
    for (int ks = 0; ks < 2; ++ks) {
      bf16x8 af[2];
#pragma unroll
      for (int mt = 0; mt < 2; ++mt) {
        int row = wm * 32 + mt * 16 + n;
        af[mt] = *reinterpret_cast<const bf16x8*>(al + row * 128 + (((ks * 4 + q) ^ (row & 7)) * 16));
      }
#pragma unroll
      for (int nt = 0; nt < 9; ++nt) {
        const u16* p0 = bl + (ks * 32 + 8 * q + (n >> 2)) * G1_BN + wn * 144 + nt * 16 + 4 * (n & 3);
        s16x4 lo = __builtin_amdgcn_ds_read_tr16_b64_v4i16((lds_s16x4_ptr)(p0));
        s16x4 hi = __builtin_amdgcn_ds_read_tr16_b64_v4i16((lds_s16x4_ptr)(p0 + 4 * G1_BN));
        bf16x8 bfr = {lo[0], lo[1], lo[2], lo[3], hi[0], hi[1], hi[2], hi[3]};
#pragma unroll
        for (int mt = 0; mt < 2; ++mt)
          acc[mt][nt] = __builtin_amdgcn_mfma_f32_16x16x32_bf16(af[mt], bfr, acc[mt][nt], 0, 0, 0);
      }
    }
    __syncthreads();
  }
  u16* ct = reinterpret_cast<u16*>(lds);
  constexpr int CS = 296;
#pragma unroll
  for (int mt = 0; mt < 2; ++mt)
#pragma unroll
    for (int nt = 0; nt < 9; ++nt)
#pragma unroll
      for (int r = 0; r < 4; ++r) {
        float hv = rbf(acc[mt][nt][r]);
        float sv = hv / (1.f + __expf(-hv));
        ct[(wm * 32 + mt * 16 + q * 4 + r) * CS + wn * 144 + nt * 16 + n] = f2bf(sv);
      }
  __syncthreads();
  for (int ch = t; ch < 128 * 36; ch += 512) {
    int row = ch / 36, cc = ch - row * 36;
    *reinterpret_cast<bf16x8*>(hmid + (size_t)(m0 + row) * F + n0 + cc * 8) =
        *reinterpret_cast<const bf16x8*>(&ct[row * CS + cc * 8]);
  }
}

constexpr int G2_BN = 144, G2_SPLIT = 4;
constexpr int G2_BBYTES = 64 * G2_BN * 2;

__global__ __launch_bounds__(512) void k3_gemm2(const u16* __restrict__ hmid, const u16* __restrict__ w2,
                                                 float* __restrict__ part) {
  __shared__ __attribute__((aligned(16))) char lds[2 * (G1_ABYTES + G2_BBYTES)];
  const int t = threadIdx.x, lane = t & 63, wv = t >> 6;
  const int bm = blockIdx.x & 15, bn = (blockIdx.x >> 4) & 3, sp = blockIdx.x >> 6;
  const int m0 = bm * 128, n0 = bn * G2_BN, k0 = sp * (F / G2_SPLIT);
  const int wm = wv & 3, ks = wv >> 2;
  const int n = lane & 15, q = lane >> 4;
  f32x4 acc[2][9];
#pragma unroll
  for (int i = 0; i < 2; ++i)
#pragma unroll
    for (int j = 0; j < 9; ++j) acc[i][j] = f32x4{0.f, 0.f, 0.f, 0.f};
  int aoff[2];
#pragma unroll
  for (int i = 0; i < 2; ++i) {
    int o = (wv * 2 + i) * 1024 + lane * 16;
    int row = o >> 7, cp = (o >> 4) & 7;
    aoff[i] = row * F + ((cp ^ (row & 7)) * 8);
  }
  int boff[3];
#pragma unroll
  for (int j = 0; j < 3; ++j) {
    int o = (wv + 8 * j) * 1024 + lane * 16;
    int row = o / 288, cb = o - row * 288;
    boff[j] = row * D + cb / 2;
  }
  const u16* abase = hmid + (size_t)m0 * F + k0;
  const u16* bbase = w2 + (size_t)k0 * D + n0;
  auto stage = [&](int kt, int buf) {
    char* al = lds + buf * (G1_ABYTES + G2_BBYTES);
    char* bl = al + G1_ABYTES;
#pragma unroll
    for (int i = 0; i < 2; ++i)
      __builtin_amdgcn_global_load_lds((const unsigned*)(abase + kt * 64 + aoff[i]),
                                       (unsigned*)(al + (wv * 2 + i) * 1024), 16, 0, 0);
#pragma unroll
    for (int j = 0; j < 3; ++j)
      if (wv + 8 * j < 18)
        __builtin_amdgcn_global_load_lds((const unsigned*)(bbase + (size_t)kt * 64 * D + boff[j]),
                                         (unsigned*)(bl + (wv + 8 * j) * 1024), 16, 0, 0);
  };
  stage(0, 0);
  __syncthreads();
  constexpr int NKT = F / G2_SPLIT / 64;
  for (int kt = 0; kt < NKT; ++kt) {
    int buf = kt & 1;
    if (kt + 1 < NKT) stage(kt + 1, buf ^ 1);
    const char* al = lds + buf * (G1_ABYTES + G2_BBYTES);
    const u16* bl = reinterpret_cast<const u16*>(al + G1_ABYTES);
    {
      bf16x8 af[2];
#pragma unroll
      for (int mt = 0; mt < 2; ++mt) {
        int row = wm * 32 + mt * 16 + n;
        af[mt] = *reinterpret_cast<const bf16x8*>(al + row * 128 + (((ks * 4 + q) ^ (row & 7)) * 16));
      }
#pragma unroll
      for (int nt = 0; nt < 9; ++nt) {
        const u16* p0 = bl + (ks * 32 + 8 * q + (n >> 2)) * G2_BN + nt * 16 + 4 * (n & 3);
        s16x4 lo = __builtin_amdgcn_ds_read_tr16_b64_v4i16((lds_s16x4_ptr)(p0));
        s16x4 hi = __builtin_amdgcn_ds_read_tr16_b64_v4i16((lds_s16x4_ptr)(p0 + 4 * G2_BN));
        bf16x8 bfr = {lo[0], lo[1], lo[2], lo[3], hi[0], hi[1], hi[2], hi[3]};
#pragma unroll
        for (int mt = 0; mt < 2; ++mt)
          acc[mt][nt] = __builtin_amdgcn_mfma_f32_16x16x32_bf16(af[mt], bfr, acc[mt][nt], 0, 0, 0);
      }
    }
    __syncthreads();
  }
  float* red = reinterpret_cast<float*>(lds);
  for (int half = 0; half < 2; ++half) {
    if (ks == 1 && (wm >> 1) == half) {
#pragma unroll
      for (int mt = 0; mt < 2; ++mt)
#pragma unroll
        for (int nt = 0; nt < 9; ++nt)
#pragma unroll
          for (int r = 0; r < 4; ++r)
            red[((wm & 1) * 32 + mt * 16 + q * 4 + r) * 144 + nt * 16 + n] = acc[mt][nt][r];
    }
    __syncthreads();
    if (ks == 0 && (wm >> 1) == half) {
#pragma unroll
      for (int mt = 0; mt < 2; ++mt)
#pragma unroll
        for (int nt = 0; nt < 9; ++nt)
#pragma unroll
          for (int r = 0; r < 4; ++r)
            part[((size_t)sp * L + m0 + wm * 32 + mt * 16 + q * 4 + r) * D + n0 + nt * 16 + n] =
                acc[mt][nt][r] + red[((wm & 1) * 32 + mt * 16 + q * 4 + r) * 144 + nt * 16 + n];
    }
    __syncthreads();
  }
}

__global__ __launch_bounds__(256) void k4_final(const float* __restrict__ part, const u16* __restrict__ x1,
                                                 const float* __restrict__ mod, u16* __restrict__ out) {
  int i = blockIdx.x * 256 + threadIdx.x;
  int e = i * 4;
  if (e >= L * D) return;
  float4 s = *reinterpret_cast<const float4*>(part + e);
#pragma unroll
  for (int p = 1; p < G2_SPLIT; ++p) {
    float4 v = *reinterpret_cast<const float4*>(part + (size_t)p * L * D + e);
    s.x += v.x; s.y += v.y; s.z += v.z; s.w += v.w;
  }
  int dcol = e % D;
  float sv[4] = {s.x, s.y, s.z, s.w};
  u16 o[4];
#pragma unroll
  for (int j = 0; j < 4; ++j) {
    float gb = mod[5 * D + dcol + j];
    o[j] = f2bf(bf2f(x1[e + j]) + rbf(gb * rbf(sv[j])));
  }
  *reinterpret_cast<uint2*>(out + e) = make_uint2(o[0] | ((uint32_t)o[1] << 16), o[2] | ((uint32_t)o[3] << 16));
}

extern "C" void kernel_launch(void* const* d_in, const int* in_sizes, int n_in, void* d_out, int out_size,
                              void* d_ws, size_t ws_size, hipStream_t stream) {
  const u16* A_u = (const u16*)d_in[0];
  const u16* Are = (const u16*)d_in[1];
  const u16* Aim = (const u16*)d_in[2];
  const u16* Bre = (const u16*)d_in[3];
  const u16* Bim = (const u16*)d_in[4];
  const u16* Cre = (const u16*)d_in[5];
  const u16* Cim = (const u16*)d_in[6];
  const u16* logdt = (const u16*)d_in[7];
  const u16* Dskip = (const u16*)d_in[8];
  const u16* w1 = (const u16*)d_in[10];
  const u16* w2 = (const u16*)d_in[11];
  const u16* c = (const u16*)d_in[12];
  const u16* wmod = (const u16*)d_in[13];
  char* ws = (char*)d_ws;
  float* mod = (float*)ws;                         ws += 16384;
  float* Kd = (float*)ws;                          ws += (size_t)D * KDS * 4;
  u16* xnT = (u16*)ws;                             ws += (size_t)L * D * 2;
  u16* xT = (u16*)ws;                              ws += (size_t)L * D * 2;
  u16* x1T = (u16*)ws;                             ws += (size_t)L * D * 2;
  u16* x1 = (u16*)ws;                              ws += (size_t)L * D * 2;
  u16* h2 = (u16*)ws;                              ws += (size_t)L * D * 2;
  u16* hmid = (u16*)ws;                            ws += (size_t)L * F * 2;
  float* part = (float*)ws;
  k0_prep<<<K0_MOD_BLOCKS + K0_STAT_BLOCKS + D, 256, 0, stream>>>(c, wmod, A_u, Are, Aim, Bre, Bim, Cre, Cim, logdt,
                                                                  mod, xnT, xT, Kd);
  k1_conv<<<D, 256, 0, stream>>>(xnT, xT, mod, Kd, Dskip, x1T);
  k1b_ln2<<<L / 32, 256, 0, stream>>>(x1T, mod, x1, h2);
  k2_gemm1<<<256, 512, 0, stream>>>(h2, w1, hmid);
  k3_gemm2<<<256, 512, 0, stream>>>(hmid, w2, part);
  k4_final<<<(L * D / 4 + 255) / 256, 256, 0, stream>>>(part, x1, mod, (u16*)d_out);
}
```

```cpp
#include <hip/hip_runtime.h>
#include <hip/hip_bf16.h>
#include <stdint.h>

typedef unsigned short u16;
using bf16x8 = __attribute__((ext_vector_type(8))) short;
using f32x4 = __attribute__((ext_vector_type(4))) float;
using f32x2 = __attribute__((ext_vector_type(2))) float;
using s16x4 = __attribute__((ext_vector_type(4))) short;
using u32x4 = __attribute__((ext_vector_type(4))) unsigned;
typedef __attribute__((address_space(3))) s16x4* lds_s16x4_ptr;

constexpr int L = 2048, D = 576, NS = 128, F = 4608;
constexpr int KDS = 656;

__device__ __forceinline__ float bf2f(u16 v) { return __uint_as_float(((uint32_t)v) << 16); }
__device__ __forceinline__ u16 f2bf(float f) {
  uint32_t u = __float_as_uint(f);
  u += 0x7FFFu + ((u >> 16) & 1u);
  return (u16)(u >> 16);
}
__device__ __forceinline__ float rbf(float f) { return bf2f(f2bf(f)); }

__device__ __forceinline__ float wave_sum(float v) {
#pragma unroll
  for (int o = 32; o > 0; o >>= 1) v += __shfl_xor(v, o);
  return v;
}
__device__ __forceinline__ void fast_cis(float x, float& sn, float& cs) {
  float r = x * 0.15915494309189535f;
  r = r - floorf(r);
  sn = __builtin_amdgcn_sinf(r);
  cs = __builtin_amdgcn_cosf(r);
}


__device__ __forceinline__ uint32_t lds_addr(const void* p) {
  return (uint32_t)(uintptr_t)(__attribute__((address_space(3))) const char*)p;
}
template <int O1, int O2>
__device__ __forceinline__ void tr_read2(uint32_t addr, s16x4& lo, s16x4& hi) {
  asm volatile("ds_read_b64_tr_b16 %0, %2 offset:%3\n\tds_read_b64_tr_b16 %1, %2 offset:%4"
               : "=v"(lo), "=v"(hi) : "v"(addr), "i"(O1), "i"(O2));
}
template <int NT, int ROWB>
struct TrLoop {
  static __device__ __forceinline__ void run(uint32_t addr, s16x4 (&lo)[9], s16x4 (&hi)[9]) {
    tr_read2<NT * 32, NT * 32 + 4 * ROWB>(addr, lo[NT], hi[NT]);
    TrLoop<NT + 1, ROWB>::run(addr, lo, hi);
  }
};
template <int ROWB>
struct TrLoop<9, ROWB> {
  static __device__ __forceinline__ void run(uint32_t, s16x4 (&)[9], s16x4 (&)[9]) {}
};

__device__ __forceinline__ int kidx_of(int l) {
  int lv = (int)rbf((float)l);
  if (lv < 256) return lv;
  if (lv < 512) return 256 + ((lv - 256) >> 1);
  if (lv < 1024) return 384 + ((lv - 512) >> 2);
  return 512 + ((lv - 1024) >> 3);
}

constexpr int K0_MOD_BLOCKS = 54, K0_STAT_BLOCKS = 128;

__global__ __launch_bounds__(256) void k0_prep(const u16* __restrict__ c, const u16* __restrict__ wmod,
                                                const u16* __restrict__ x, const u16* __restrict__ Are,
                                                const u16* __restrict__ Aim, const u16* __restrict__ Bre,
                                                const u16* __restrict__ Bim, const u16* __restrict__ Cre,
                                                const u16* __restrict__ Cim, const u16* __restrict__ logdt,
                                                float* __restrict__ mod, u16* __restrict__ xnT,
                                                u16* __restrict__ xT, float* __restrict__ Kd) {
  __shared__ __attribute__((aligned(16))) char smraw[2 * 16 * 584 * 2];
  const int b = (blockIdx.x < D) ? (blockIdx.x + K0_MOD_BLOCKS + K0_STAT_BLOCKS) : (blockIdx.x - D);
  const int t = threadIdx.x;
  const int lane = t & 63, wv = t >> 6;
  if (b < K0_MOD_BLOCKS) {
    float* s_s = reinterpret_cast<float*>(smraw);
    float* red = s_s + 576;
    for (int i = t; i < D; i += 256) {
      float cv = bf2f(c[i]);
      s_s[i] = rbf(cv / (1.f + __expf(-cv)));
    }
    __syncthreads();
    int cg = t & 7, rg = t >> 3;
    float acc[8];
#pragma unroll
    for (int j = 0; j < 8; ++j) acc[j] = 0.f;
#pragma unroll 6
    for (int r = rg * 18; r < rg * 18 + 18; ++r) {
      bf16x8 w = *reinterpret_cast<const bf16x8*>(wmod + (size_t)r * (6 * D) + b * 64 + cg * 8);
      float s = s_s[r];
#pragma unroll
      for (int j = 0; j < 8; ++j) acc[j] += s * bf2f((u16)w[j]);
    }
#pragma unroll
    for (int j = 0; j < 8; ++j) red[rg * 64 + cg * 8 + j] = acc[j];
    __syncthreads();
    if (t < 64) {
      float s = 0.f;
      for (int r = 0; r < 32; ++r) s += red[r * 64 + t];
      mod[b * 64 + t] = rbf(s);
    }
  } else if (b < K0_MOD_BLOCKS + K0_STAT_BLOCKS) {
    u16* tn = reinterpret_cast<u16*>(smraw);
    u16* tx = tn + 16 * 584;
    int r0 = (b - K0_MOD_BLOCKS) * 16;
    for (int rr = wv * 4; rr < wv * 4 + 4; ++rr) {
      int r = r0 + rr;
      u16 raw[9];
      float v[9];
      float s = 0.f;
#pragma unroll
      for (int j = 0; j < 9; ++j) { raw[j] = x[(size_t)r * D + lane + 64 * j]; v[j] = bf2f(raw[j]); s += v[j]; }
      s = wave_sum(s);
      float m = s * (1.f / D);
      float q = 0.f;
#pragma unroll
      for (int j = 0; j < 9; ++j) { float dd = v[j] - m; q += dd * dd; }
      q = wave_sum(q);
      float var = rbf(q * (1.f / D));
      float sq = rbf(sqrtf(rbf(var + 1e-5f)));
      float mb = rbf(m);
#pragma unroll
      for (int j = 0; j < 9; ++j) {
        tn[rr * 584 + lane + 64 * j] = f2bf(rbf(v[j] - mb) / sq);
        tx[rr * 584 + lane + 64 * j] = raw[j];
      }
    }
    __syncthreads();
    for (int i = t; i < D * 2; i += 256) {
      int dch = i >> 1, part = i & 1;
      u16 on[8], ox[8];
#pragma unroll
      for (int j = 0; j < 8; ++j) { on[j] = tn[(part * 8 + j) * 584 + dch]; ox[j] = tx[(part * 8 + j) * 584 + dch]; }
      *reinterpret_cast<bf16x8*>(xnT + (size_t)dch * L + r0 + part * 8) = *reinterpret_cast<bf16x8*>(on);
      *reinterpret_cast<bf16x8*>(xT + (size_t)dch * L + r0 + part * 8) = *reinterpret_cast<bf16x8*>(ox);
    }
  } else {
    const int d = b - K0_MOD_BLOCKS - K0_STAT_BLOCKS;
    float* red = reinterpret_cast<float*>(smraw);
    float* pr = red + 4 * 656;
    if (t < NS) {
      float dt = rbf(__expf(bf2f(logdt[d])));
      float ar = dt * bf2f(Are[d * NS + t]), ai = dt * bf2f(Aim[d * NS + t]);
      float br = dt * bf2f(Bre[d * NS + t]), bi = dt * bf2f(Bim[d * NS + t]);
      float cr = bf2f(Cre[d * NS + t]), ci = bf2f(Cim[d * NS + t]);
      pr[t * 12 + 0] = ar; pr[t * 12 + 1] = ai;
      pr[t * 12 + 2] = cr * br - ci * bi; pr[t * 12 + 3] = cr * bi + ci * br;
#pragma unroll
      for (int s = 0; s < 4; ++s) {
        float st = (float)(1 << s);
        float e = __expf(ar * st);
        float sn, cs;
        fast_cis(ai * st, sn, cs);
        pr[t * 12 + 4 + 2 * s] = e * cs; pr[t * 12 + 5 + 2 * s] = e * sn;
      }
    }
    __syncthreads();
    if (lane < 41) {
      int c_ = lane, seg, lv0;
      if (c_ < 16) { seg = 0; lv0 = 16 * c_; }
      else if (c_ < 24) { seg = 1; lv0 = 256 + 32 * (c_ - 16); }
      else if (c_ < 32) { seg = 2; lv0 = 512 + 64 * (c_ - 24); }
      else { seg = 3; lv0 = 1024 + 128 * (c_ - 32); }
      const float flv = (float)lv0;
      float acc[16];
#pragma unroll
      for (int i = 0; i < 16; ++i) acc[i] = 0.f;
      for (int n = wv * 32; n < wv * 32 + 32; ++n) {
        f32x4 p0 = *reinterpret_cast<const f32x4*>(&pr[n * 12]);
        f32x2 E = *reinterpret_cast<const f32x2*>(&pr[n * 12 + 4 + 2 * seg]);
        f32x2 En = {-E.y, E.x};
        float e = __expf(p0.x * flv);
        float sn, cs;
        fast_cis(p0.y * flv, sn, cs);
        float pr_ = e * cs, pi_ = e * sn;
        f32x2 S = {p0.z * pr_ - p0.w * pi_, p0.z * pi_ + p0.w * pr_};
#pragma unroll
        for (int i = 0; i < 16; ++i) {
          acc[i] += S.x;
          f32x2 sx = {S.x, S.x}, sy = {S.y, S.y};
          S = sx * E + sy * En;
        }
      }
#pragma unroll
      for (int i = 0; i < 16; ++i) red[wv * 656 + c_ * 16 + i] = acc[i];
    }
    __syncthreads();
    for (int i = t; i < 641; i += 256) Kd[d * KDS + i] = red[i] + red[656 + i] + red[2 * 656 + i] + red[3 * 656 + i];
  }
}

constexpr int HCS = 2192;
constexpr int HOFF = 2048;
constexpr int KROW = 72;

__global__ __launch_bounds__(256) void k1_conv(const u16* __restrict__ xnT, const u16* __restrict__ xT,
                                                const float* __restrict__ mod, const float* __restrict__ Kd,
                                                const u16* __restrict__ Dskip, u16* __restrict__ x1T) {
  __shared__ __attribute__((aligned(16))) u16 hc[8 * HCS];
  __shared__ __attribute__((aligned(16))) u16 kp[2 * 64 * KROW];
  __shared__ __attribute__((aligned(16))) u16 hr[HCS + 16];
  const int d = blockIdx.x, t = threadIdx.x, lane = t & 63, wv = t >> 6;
  const float ca1 = rbf(1.f + mod[D + d]), sa = mod[d], ga = mod[2 * D + d];
  u16 hv[8];
  {
    bf16x8 xn = *reinterpret_cast<const bf16x8*>(xnT + (size_t)d * L + 8 * t);
#pragma unroll
    for (int j = 0; j < 8; ++j) hv[j] = f2bf(rbf(bf2f((u16)xn[j]) * ca1) + sa);
  }
  {
    float kv[8];
#pragma unroll
    for (int j = 0; j < 8; ++j) kv[j] = Kd[d * KDS + kidx_of(8 * t + j)];
    u16 hi[8], lo[8];
#pragma unroll
    for (int j = 0; j < 8; ++j) { hi[j] = f2bf(kv[j]); lo[j] = f2bf(kv[j] - bf2f(hi[j])); }
    int l0 = 8 * t;
    *reinterpret_cast<bf16x8*>(&kp[(32 + (l0 >> 6)) * KROW + (l0 & 63)]) = *reinterpret_cast<bf16x8*>(hi);
    *reinterpret_cast<bf16x8*>(&kp[(96 + (l0 >> 6)) * KROW + (l0 & 63)]) = *reinterpret_cast<bf16x8*>(lo);
  }
  for (int i = t; i < 32 * KROW / 8; i += 256) {
    *reinterpret_cast<bf16x8*>(&kp[i * 8]) = bf16x8{0, 0, 0, 0, 0, 0, 0, 0};
    *reinterpret_cast<bf16x8*>(&kp[64 * KROW + i * 8]) = bf16x8{0, 0, 0, 0, 0, 0, 0, 0};
  }
  {
    u16 rv[8];
#pragma unroll
    for (int j = 0; j < 8; ++j) rv[j] = hv[7 - j];
    int i0 = 8 + HOFF - 8 * t - 7;
#pragma unroll
    for (int j = 0; j < 8; ++j) hr[i0 + j] = rv[j];
  }
  if (t < 8) hr[t] = 0;
  if (t == 8) hr[8] = 0;
  for (int i = 8 + HOFF + 1 + t; i < HCS + 16; i += 256) hr[i] = 0;
  __syncthreads();
  for (int u = t; u < HCS / 8; u += 256) {
    bf16x8 a0 = *reinterpret_cast<const bf16x8*>(&hr[8 * u]);
    bf16x8 a1 = *reinterpret_cast<const bf16x8*>(&hr[8 * u + 8]);
    u16 w[16];
#pragma unroll
    for (int j = 0; j < 8; ++j) { w[j] = (u16)a0[j]; w[8 + j] = (u16)a1[j]; }
#pragma unroll
    for (int c_ = 0; c_ < 8; ++c_) {
      bf16x8 o;
#pragma unroll
      for (int j = 0; j < 8; ++j) o[j] = (short)w[8 + j - c_];
      *reinterpret_cast<bf16x8*>(&hc[c_ * HCS + 8 * u]) = o;
    }
  }
  __syncthreads();
  const int n = lane & 15, q = lane >> 4, nh = n >> 3, cc = n & 7;
  f32x4 acc[2][4];
#pragma unroll
  for (int m = 0; m < 2; ++m)
#pragma unroll
    for (int j = 0; j < 4; ++j) acc[m][j] = f32x4{0.f, 0.f, 0.f, 0.f};
  for (int dl = wv; dl < 32; dl += 4) {
#pragma unroll
    for (int ks = 0; ks < 2; ++ks) {
      bf16x8 bfr[4];
#pragma unroll
      for (int nt = 0; nt < 4; ++nt) {
        int base = dl * 64 + nt * 16 - ks * 32 - 8 * q;
        int x0 = HOFF - base - 8 * nh;
        bfr[nt] = *reinterpret_cast<const bf16x8*>(&hc[cc * HCS + x0]);
      }
      {
        int row = 32 + 16 + n - dl;
        bf16x8 ah = *reinterpret_cast<const bf16x8*>(&kp[row * KROW + ks * 32 + 8 * q]);
        bf16x8 al = *reinterpret_cast<const bf16x8*>(&kp[(64 + row) * KROW + ks * 32 + 8 * q]);
#pragma unroll
        for (int nt = 0; nt < 4; ++nt) {
          acc[1][nt] = __builtin_amdgcn_mfma_f32_16x16x32_bf16(ah, bfr[nt], acc[1][nt], 0, 0, 0);
          acc[1][nt] = __builtin_amdgcn_mfma_f32_16x16x32_bf16(al, bfr[nt], acc[1][nt], 0, 0, 0);
        }
      }
      if (dl < 16) {
        int row = 32 + n - dl;
        bf16x8 ah = *reinterpret_cast<const bf16x8*>(&kp[row * KROW + ks * 32 + 8 * q]);
        bf16x8 al = *reinterpret_cast<const bf16x8*>(&kp[(64 + row) * KROW + ks * 32 + 8 * q]);
#pragma unroll
        for (int nt = 0; nt < 4; ++nt) {
          acc[0][nt] = __builtin_amdgcn_mfma_f32_16x16x32_bf16(ah, bfr[nt], acc[0][nt], 0, 0, 0);
          acc[0][nt] = __builtin_amdgcn_mfma_f32_16x16x32_bf16(al, bfr[nt], acc[0][nt], 0, 0, 0);
        }
      }
    }
  }
  __syncthreads();
  float* red = reinterpret_cast<float*>(hc);
#pragma unroll
  for (int m = 0; m < 2; ++m)
#pragma unroll
    for (int nt = 0; nt < 4; ++nt)
#pragma unroll
      for (int r = 0; r < 4; ++r) red[wv * L + (m * 16 + q * 4 + r) * 64 + nt * 16 + n] = acc[m][nt][r];
  __syncthreads();
  const float dsk = bf2f(Dskip[d]);
  {
    bf16x8 xr = *reinterpret_cast<const bf16x8*>(xT + (size_t)d * L + 8 * t);
    u16 o[8];
#pragma unroll
    for (int j = 0; j < 8; ++j) {
      int l = 8 * t + j;
      float y = rbf(red[l] + red[L + l] + red[2 * L + l] + red[3 * L + l]);
      float ya = rbf(y + rbf(bf2f(hv[j]) * dsk));
      o[j] = f2bf(bf2f((u16)xr[j]) + rbf(ga * ya));
    }
    *reinterpret_cast<bf16x8*>(x1T + (size_t)d * L + 8 * t) = *reinterpret_cast<bf16x8*>(o);
  }
}

__global__ __launch_bounds__(256) void k1b_ln2(const u16* __restrict__ x1T, const float* __restrict__ mod,
                                                u16* __restrict__ x1, u16* __restrict__ h2) {
  __shared__ __attribute__((aligned(16))) u16 tl[8 * 584];
  const int t = threadIdx.x, lane = t & 63, wv = t >> 6;
  const int r0 = blockIdx.x * 8;
  for (int dch = t; dch < D; dch += 256) {
    bf16x8 v = *reinterpret_cast<const bf16x8*>(x1T + (size_t)dch * L + r0);
#pragma unroll
    for (int j = 0; j < 8; ++j) tl[j * 584 + dch] = (u16)v[j];
  }
  __syncthreads();
  for (int rr = wv * 2; rr < wv * 2 + 2; ++rr) {
    float v[9];
    float s = 0.f;
#pragma unroll
    for (int j = 0; j < 9; ++j) { v[j] = bf2f(tl[rr * 584 + lane + 64 * j]); s += v[j]; }
    s = wave_sum(s);
    float m = s * (1.f / D);
    float q = 0.f;
#pragma unroll
    for (int j = 0; j < 9; ++j) { float dd = v[j] - m; q += dd * dd; }
    q = wave_sum(q);
    float var = rbf(q * (1.f / D));
    float sq = rbf(sqrtf(rbf(var + 1e-5f)));
    float mb = rbf(m);
    size_t ro = (size_t)(r0 + rr) * D;
#pragma unroll
    for (int j = 0; j < 9; ++j) {
      int k = lane + 64 * j;
      float hv = rbf(rbf(rbf(v[j] - mb) / sq) * rbf(1.f + mod[4 * D + k])) + mod[3 * D + k];
      h2[ro + k] = f2bf(hv);
      x1[ro + k] = f2bf(v[j]);
    }
  }
}

constexpr int G1_BM = 128, G1_BN = 288;
constexpr int G1_ABYTES = G1_BM * 64 * 2, G1_BBYTES = 64 * G1_BN * 2;

constexpr int G1_NST = 3;
__global__ __launch_bounds__(512) void k2_gemm1(const u16* __restrict__ h2, const u16* __restrict__ w1,
                                                 u16* __restrict__ hmid) {
  __shared__ __attribute__((aligned(16))) char lds[G1_NST * (G1_ABYTES + G1_BBYTES)];
  const int t = threadIdx.x, lane = t & 63, wv = t >> 6;
  const int bm = blockIdx.x & 15, bn = blockIdx.x >> 4;
  const int m0 = bm * G1_BM, n0 = bn * G1_BN;
  const int wm = wv & 3, wn = wv >> 2;
  const int n = lane & 15, q = lane >> 4;
  f32x4 acc[2][9];
#pragma unroll
  for (int i = 0; i < 2; ++i)
#pragma unroll
    for (int j = 0; j < 9; ++j) acc[i][j] = f32x4{0.f, 0.f, 0.f, 0.f};

  int aoff[2];
#pragma unroll
  for (int i = 0; i < 2; ++i) {
    int o = (wv * 2 + i) * 1024 + lane * 16;
    int row = o >> 7, cp = (o >> 4) & 7;
    aoff[i] = row * D + ((cp ^ (row & 7)) * 8);
  }
  int boff[5], bdst[5];
#pragma unroll
  for (int j = 0; j < 5; ++j) {
    int idx = wv + 8 * j;
    if (idx >= 36) idx -= 8;
    int o = idx * 1024 + lane * 16;
    int row = o / 576, cb = o - row * 576;
    boff[j] = row * F + cb / 2;
    bdst[j] = idx * 1024;
  }
  const u16* abase = h2 + (size_t)m0 * D;
  const u16* bbase = w1 + n0;
  auto stage = [&](int kt, int buf) {
    char* al = lds + buf * (G1_ABYTES + G1_BBYTES);
    char* bl = al + G1_ABYTES;
#pragma unroll
    for (int i = 0; i < 2; ++i)
      __builtin_amdgcn_global_load_lds((const unsigned*)(abase + kt * 64 + aoff[i]),
                                       (unsigned*)(al + (wv * 2 + i) * 1024), 16, 0, 0);
#pragma unroll
    for (int j = 0; j < 5; ++j)
      __builtin_amdgcn_global_load_lds((const unsigned*)(bbase + (size_t)kt * 64 * F + boff[j]),
                                       (unsigned*)(bl + bdst[j]), 16, 0, 0);
  };
  stage(0, 0);
  stage(1, 1);
  int buf = 0;
  for (int kt = 0; kt < 9; ++kt) {
    if (kt < 8) asm volatile("s_waitcnt vmcnt(7)" ::: "memory");
    else asm volatile("s_waitcnt vmcnt(0)" ::: "memory");
    __builtin_amdgcn_s_barrier();
    if (kt + 2 < 9) stage(kt + 2, buf == 0 ? 2 : buf - 1);
    const char* al = lds + buf * (G1_ABYTES + G1_BBYTES);
    const u16* bl = reinterpret_cast<const u16*>(al + G1_ABYTES);
#pragma unroll
    for (int ks = 0; ks < 2; ++ks) {
      bf16x8 af[2];
#pragma unroll
      for (int mt = 0; mt < 2; ++mt) {
        int row = wm * 32 + mt * 16 + n;
        af[mt] = *reinterpret_cast<const bf16x8*>(al + row * 128 + (((ks * 4 + q) ^ (row & 7)) * 16));
      }
      s16x4 lo[9], hi[9];
      TrLoop<0, G1_BN * 2>::run(lds_addr(bl + (ks * 32 + 8 * q + (n >> 2)) * G1_BN + wn * 144 + 4 * (n & 3)), lo, hi);
      asm volatile("s_waitcnt lgkmcnt(0)" ::: "memory");
      __builtin_amdgcn_sched_barrier(0);
#pragma unroll
      for (int nt = 0; nt < 9; ++nt) {
        bf16x8 bfr = {lo[nt][0], lo[nt][1], lo[nt][2], lo[nt][3], hi[nt][0], hi[nt][1], hi[nt][2], hi[nt][3]};
#pragma unroll
        for (int mt = 0; mt < 2; ++mt)
          acc[mt][nt] = __builtin_amdgcn_mfma_f32_16x16x32_bf16(af[mt], bfr, acc[mt][nt], 0, 0, 0);
      }
    }
    buf = (buf == 2) ? 0 : buf + 1;
  }
  __syncthreads();
  u16* ct = reinterpret_cast<u16*>(lds);
  constexpr int CS = 296;
#pragma unroll
  for (int mt = 0; mt < 2; ++mt)
#pragma unroll
    for (int nt = 0; nt < 9; ++nt)
#pragma unroll
      for (int r = 0; r < 4; ++r) {
        float hv = rbf(acc[mt][nt][r]);
        float sv = hv / (1.f + __expf(-hv));
        ct[(wm * 32 + mt * 16 + q * 4 + r) * CS + wn * 144 + nt * 16 + n] = f2bf(sv);
      }
  __syncthreads();
  for (int ch = t; ch < 128 * 36; ch += 512) {
    int row = ch / 36, cc = ch - row * 36;
    *reinterpret_cast<bf16x8*>(hmid + (size_t)(m0 + row) * F + n0 + cc * 8) =
        *reinterpret_cast<const bf16x8*>(&ct[row * CS + cc * 8]);
  }
}

constexpr int G2_BN = 144, G2_SPLIT = 4;
constexpr int G2_BBYTES = 64 * G2_BN * 2;

constexpr int G2_NST = 4;
__global__ __launch_bounds__(512) void k3_gemm2(const u16* __restrict__ hmid, const u16* __restrict__ w2,
                                                 float* __restrict__ part) {
  __shared__ __attribute__((aligned(16))) char lds[G2_NST * (G1_ABYTES + G2_BBYTES)];
  const int t = threadIdx.x, lane = t & 63, wv = t >> 6;
  const int bm = blockIdx.x & 15, bn = (blockIdx.x >> 4) & 3, sp = blockIdx.x >> 6;
  const int m0 = bm * 128, n0 = bn * G2_BN, k0 = sp * (F / G2_SPLIT);
  const int wm = wv & 3, ks = wv >> 2;
  const int n = lane & 15, q = lane >> 4;
  f32x4 acc[2][9];
#pragma unroll
  for (int i = 0; i < 2; ++i)
#pragma unroll
    for (int j = 0; j < 9; ++j) acc[i][j] = f32x4{0.f, 0.f, 0.f, 0.f};
  int aoff[2];
#pragma unroll
  for (int i = 0; i < 2; ++i) {
    int o = (wv * 2 + i) * 1024 + lane * 16;
    int row = o >> 7, cp = (o >> 4) & 7;
    aoff[i] = row * F + ((cp ^ (row & 7)) * 8);
  }
  int boff[3], bdst[3];
#pragma unroll
  for (int j = 0; j < 3; ++j) {
    int idx = wv + 8 * j;
    if (idx >= 18) idx -= 8;
    int o = idx * 1024 + lane * 16;
    int row = o / 288, cb = o - row * 288;
    boff[j] = row * D + cb / 2;
    bdst[j] = idx * 1024;
  }
  const u16* abase = hmid + (size_t)m0 * F + k0;
  const u16* bbase = w2 + (size_t)k0 * D + n0;
  auto stage = [&](int kt, int buf) {
    char* al = lds + buf * (G1_ABYTES + G2_BBYTES);
    char* bl = al + G1_ABYTES;
#pragma unroll
    for (int i = 0; i < 2; ++i)
      __builtin_amdgcn_global_load_lds((const unsigned*)(abase + kt * 64 + aoff[i]),
                                       (unsigned*)(al + (wv * 2 + i) * 1024), 16, 0, 0);
#pragma unroll
    for (int j = 0; j < 3; ++j)
      __builtin_amdgcn_global_load_lds((const unsigned*)(bbase + (size_t)kt * 64 * D + boff[j]),
                                       (unsigned*)(bl + bdst[j]), 16, 0, 0);
  };
  constexpr int NKT = F / G2_SPLIT / 64;
  stage(0, 0);
  stage(1, 1);
  stage(2, 2);
  for (int kt = 0; kt < NKT; ++kt) {
    if (kt < NKT - 2) asm volatile("s_waitcnt vmcnt(10)" ::: "memory");
    else if (kt == NKT - 2) asm volatile("s_waitcnt vmcnt(5)" ::: "memory");
    else asm volatile("s_waitcnt vmcnt(0)" ::: "memory");
    __builtin_amdgcn_s_barrier();
    if (kt + 3 < NKT) stage(kt + 3, (kt + 3) & 3);
    const char* al = lds + (kt & 3) * (G1_ABYTES + G2_BBYTES);
    const u16* bl = reinterpret_cast<const u16*>(al + G1_ABYTES);
    {
      bf16x8 af[2];
#pragma unroll
      for (int mt = 0; mt < 2; ++mt) {
        int row = wm * 32 + mt * 16 + n;
        af[mt] = *reinterpret_cast<const bf16x8*>(al + row * 128 + (((ks * 4 + q) ^ (row & 7)) * 16));
      }
      s16x4 lo[9], hi[9];
      TrLoop<0, G2_BN * 2>::run(lds_addr(bl + (ks * 32 + 8 * q + (n >> 2)) * G2_BN + 4 * (n & 3)), lo, hi);
      asm volatile("s_waitcnt lgkmcnt(0)" ::: "memory");
      __builtin_amdgcn_sched_barrier(0);
#pragma unroll
      for (int nt = 0; nt < 9; ++nt) {
        bf16x8 bfr = {lo[nt][0], lo[nt][1], lo[nt][2], lo[nt][3], hi[nt][0], hi[nt][1], hi[nt][2], hi[nt][3]};
#pragma unroll
        for (int mt = 0; mt < 2; ++mt)
          acc[mt][nt] = __builtin_amdgcn_mfma_f32_16x16x32_bf16(af[mt], bfr, acc[mt][nt], 0, 0, 0);
      }
    }
  }
  __syncthreads();
  float* red = reinterpret_cast<float*>(lds);
  for (int half = 0; half < 2; ++half) {
    if (ks == 1 && (wm >> 1) == half) {
#pragma unroll
      for (int mt = 0; mt < 2; ++mt)
#pragma unroll
        for (int nt = 0; nt < 9; ++nt)
#pragma unroll
          for (int r = 0; r < 4; ++r)
            red[((wm & 1) * 32 + mt * 16 + q * 4 + r) * 144 + nt * 16 + n] = acc[mt][nt][r];
    }
    __syncthreads();
    if (ks == 0 && (wm >> 1) == half) {
#pragma unroll
      for (int mt = 0; mt < 2; ++mt)
#pragma unroll
        for (int nt = 0; nt < 9; ++nt)
#pragma unroll
          for (int r = 0; r < 4; ++r)
            part[((size_t)sp * L + m0 + wm * 32 + mt * 16 + q * 4 + r) * D + n0 + nt * 16 + n] =
                acc[mt][nt][r] + red[((wm & 1) * 32 + mt * 16 + q * 4 + r) * 144 + nt * 16 + n];
    }
    __syncthreads();
  }
}

__global__ __launch_bounds__(256) void k4_final(const float* __restrict__ part, const u16* __restrict__ x1,
                                                 const float* __restrict__ mod, u16* __restrict__ out) {
  int i = blockIdx.x * 256 + threadIdx.x;
  int e = i * 4;
  if (e >= L * D) return;
  float4 s = *reinterpret_cast<const float4*>(part + e);
#pragma unroll
  for (int p = 1; p < G2_SPLIT; ++p) {
    float4 v = *reinterpret_cast<const float4*>(part + (size_t)p * L * D + e);
    s.x += v.x; s.y += v.y; s.z += v.z; s.w += v.w;
  }
  int dcol = e % D;
  float sv[4] = {s.x, s.y, s.z, s.w};
  u16 o[4];
#pragma unroll
  for (int j = 0; j < 4; ++j) {
    float gb = mod[5 * D + dcol + j];
    o[j] = f2bf(bf2f(x1[e + j]) + rbf(gb * rbf(sv[j])));
  }
  *reinterpret_cast<uint2*>(out + e) = make_uint2(o[0] | ((uint32_t)o[1] << 16), o[2] | ((uint32_t)o[3] << 16));
}

extern "C" void kernel_launch(void* const* d_in, const int* in_sizes, int n_in, void* d_out, int out_size,
                              void* d_ws, size_t ws_size, hipStream_t stream) {
  const u16* A_u = (const u16*)d_in[0];
  const u16* Are = (const u16*)d_in[1];
  const u16* Aim = (const u16*)d_in[2];
  const u16* Bre = (const u16*)d_in[3];
  const u16* Bim = (const u16*)d_in[4];
  const u16* Cre = (const u16*)d_in[5];
  const u16* Cim = (const u16*)d_in[6];
  const u16* logdt = (const u16*)d_in[7];
  const u16* Dskip = (const u16*)d_in[8];
  const u16* w1 = (const u16*)d_in[10];
  const u16* w2 = (const u16*)d_in[11];
  const u16* c = (const u16*)d_in[12];
  const u16* wmod = (const u16*)d_in[13];
  char* ws = (char*)d_ws;
  float* mod = (float*)ws;                         ws += 16384;
  float* Kd = (float*)ws;                          ws += (size_t)D * KDS * 4;
  u16* xnT = (u16*)ws;                             ws += (size_t)L * D * 2;
  u16* xT = (u16*)ws;                              ws += (size_t)L * D * 2;
  u16* x1T = (u16*)ws;                             ws += (size_t)L * D * 2;
  u16* x1 = (u16*)ws;                              ws += (size_t)L * D * 2;
  u16* h2 = (u16*)ws;                              ws += (size_t)L * D * 2;
  u16* hmid = (u16*)ws;                            ws += (size_t)L * F * 2;
  float* part = (float*)ws;
  k0_prep<<<K0_MOD_BLOCKS + K0_STAT_BLOCKS + D, 256, 0, stream>>>(c, wmod, A_u, Are, Aim, Bre, Bim, Cre, Cim, logdt,
                                                                  mod, xnT, xT, Kd);
  k1_conv<<<D, 256, 0, stream>>>(xnT, xT, mod, Kd, Dskip, x1T);
  k1b_ln2<<<L / 8, 256, 0, stream>>>(x1T, mod, x1, h2);
  k2_gemm1<<<256, 512, 0, stream>>>(h2, w1, hmid);
  k3_gemm2<<<256, 512, 0, stream>>>(hmid, w2, part);
  k4_final<<<(L * D / 4 + 255) / 256, 256, 0, stream>>>(part, x1, mod, (u16*)d_out);
}
```

```cpp
#include <hip/hip_runtime.h>
#include <hip/hip_bf16.h>
#include <stdint.h>

typedef unsigned short u16;
using bf16x8 = __attribute__((ext_vector_type(8))) short;
using f32x4 = __attribute__((ext_vector_type(4))) float;
using f32x2 = __attribute__((ext_vector_type(2))) float;
using s16x4 = __attribute__((ext_vector_type(4))) short;
using u32x4 = __attribute__((ext_vector_type(4))) unsigned;
typedef __attribute__((address_space(3))) s16x4* lds_s16x4_ptr;

constexpr int L = 2048, D = 576, NS = 128, F = 4608;
constexpr int KDS = 656;

__device__ __forceinline__ float bf2f(u16 v) { return __uint_as_float(((uint32_t)v) << 16); }
__device__ __forceinline__ u16 f2bf(float f) {
  uint32_t u = __float_as_uint(f);
  u += 0x7FFFu + ((u >> 16) & 1u);
  return (u16)(u >> 16);
}
__device__ __forceinline__ float rbf(float f) { return bf2f(f2bf(f)); }

__device__ __forceinline__ float wave_sum(float v) {
#pragma unroll
  for (int o = 32; o > 0; o >>= 1) v += __shfl_xor(v, o);
  return v;
}
__device__ __forceinline__ void fast_cis(float x, float& sn, float& cs) {
  float r = x * 0.15915494309189535f;
  r = r - floorf(r);
  sn = __builtin_amdgcn_sinf(r);
  cs = __builtin_amdgcn_cosf(r);
}


__device__ __forceinline__ uint32_t lds_addr(const void* p) {
  return (uint32_t)(uintptr_t)(__attribute__((address_space(3))) const char*)p;
}
template <int O1, int O2>
__device__ __forceinline__ void tr_read2(uint32_t addr, s16x4& lo, s16x4& hi) {
  asm volatile("ds_read_b64_tr_b16 %0, %2 offset:%3\n\tds_read_b64_tr_b16 %1, %2 offset:%4"
               : "=&v"(lo), "=&v"(hi) : "v"(addr), "i"(O1), "i"(O2));
}
template <int NT, int ROWB>
struct TrLoop {
  static __device__ __forceinline__ void run(uint32_t addr, uint32_t addr1, s16x4 (&lo)[9], s16x4 (&hi)[9]) {
    tr_read2<NT * 32, NT * 32 + 4 * ROWB>((ROWB == 288 && NT == 8) ? ((addr + addr1) >> 1) : ((NT & 1) ? addr1 : addr), lo[NT], hi[NT]);
    TrLoop<NT + 1, ROWB>::run(addr, addr1, lo, hi);
  }
};
template <int ROWB>
struct TrLoop<9, ROWB> {
  static __device__ __forceinline__ void run(uint32_t, uint32_t, s16x4 (&)[9], s16x4 (&)[9]) {}
};

__device__ __forceinline__ int kidx_of(int l) {
  int lv = (int)rbf((float)l);
  if (lv < 256) return lv;
  if (lv < 512) return 256 + ((lv - 256) >> 1);
  if (lv < 1024) return 384 + ((lv - 512) >> 2);
  return 512 + ((lv - 1024) >> 3);
}

constexpr int K0_MOD_BLOCKS = 54, K0_STAT_BLOCKS = 128;

__global__ __launch_bounds__(256) void k0_prep(const u16* __restrict__ c, const u16* __restrict__ wmod,
                                                const u16* __restrict__ x, const u16* __restrict__ Are,
                                                const u16* __restrict__ Aim, const u16* __restrict__ Bre,
                                                const u16* __restrict__ Bim, const u16* __restrict__ Cre,
                                                const u16* __restrict__ Cim, const u16* __restrict__ logdt,
                                                float* __restrict__ mod, u16* __restrict__ xnT,
                                                u16* __restrict__ xT, float* __restrict__ Kd) {
  __shared__ __attribute__((aligned(16))) char smraw[2 * 16 * 584 * 2];
  const int b = (blockIdx.x < D) ? (blockIdx.x + K0_MOD_BLOCKS + K0_STAT_BLOCKS) : (blockIdx.x - D);
  const int t = threadIdx.x;
  const int lane = t & 63, wv = t >> 6;
  if (b < K0_MOD_BLOCKS) {
    float* s_s = reinterpret_cast<float*>(smraw);
    float* red = s_s + 576;
    for (int i = t; i < D; i += 256) {
      float cv = bf2f(c[i]);
      s_s[i] = rbf(cv / (1.f + __expf(-cv)));
    }
    __syncthreads();
    int cg = t & 7, rg = t >> 3;
    float acc[8];
#pragma unroll
    for (int j = 0; j < 8; ++j) acc[j] = 0.f;
#pragma unroll 6
    for (int r = rg * 18; r < rg * 18 + 18; ++r) {
      bf16x8 w = *reinterpret_cast<const bf16x8*>(wmod + (size_t)r * (6 * D) + b * 64 + cg * 8);
      float s = s_s[r];
#pragma unroll
      for (int j = 0; j < 8; ++j) acc[j] += s * bf2f((u16)w[j]);
    }
#pragma unroll
    for (int j = 0; j < 8; ++j) red[rg * 64 + cg * 8 + j] = acc[j];
    __syncthreads();
    if (t < 64) {
      float s = 0.f;
      for (int r = 0; r < 32; ++r) s += red[r * 64 + t];
      mod[b * 64 + t] = rbf(s);
    }
  } else if (b < K0_MOD_BLOCKS + K0_STAT_BLOCKS) {
    u16* tn = reinterpret_cast<u16*>(smraw);
    u16* tx = tn + 16 * 584;
    int r0 = (b - K0_MOD_BLOCKS) * 16;
    for (int rr = wv * 4; rr < wv * 4 + 4; ++rr) {
      int r = r0 + rr;
      u16 raw[9];
      float v[9];
      float s = 0.f;
#pragma unroll
      for (int j = 0; j < 9; ++j) { raw[j] = x[(size_t)r * D + lane + 64 * j]; v[j] = bf2f(raw[j]); s += v[j]; }
      s = wave_sum(s);
      float m = s * (1.f / D);
      float q = 0.f;
#pragma unroll
      for (int j = 0; j < 9; ++j) { float dd = v[j] - m; q += dd * dd; }
      q = wave_sum(q);
      float var = rbf(q * (1.f / D));
      float sq = rbf(sqrtf(rbf(var + 1e-5f)));
      float mb = rbf(m);
#pragma unroll
      for (int j = 0; j < 9; ++j) {
        tn[rr * 584 + lane + 64 * j] = f2bf(rbf(v[j] - mb) / sq);
        tx[rr * 584 + lane + 64 * j] = raw[j];
      }
    }
    __syncthreads();
    for (int i = t; i < D * 2; i += 256) {
      int dch = i >> 1, part = i & 1;
      u16 on[8], ox[8];
#pragma unroll
      for (int j = 0; j < 8; ++j) { on[j] = tn[(part * 8 + j) * 584 + dch]; ox[j] = tx[(part * 8 + j) * 584 + dch]; }
      *reinterpret_cast<bf16x8*>(xnT + (size_t)dch * L + r0 + part * 8) = *reinterpret_cast<bf16x8*>(on);
      *reinterpret_cast<bf16x8*>(xT + (size_t)dch * L + r0 + part * 8) = *reinterpret_cast<bf16x8*>(ox);
    }
  } else {
    const int d = b - K0_MOD_BLOCKS - K0_STAT_BLOCKS;
    float* red = reinterpret_cast<float*>(smraw);
    float* pr = red + 4 * 656;
    if (t < NS) {
      float dt = rbf(__expf(bf2f(logdt[d])));
      float ar = dt * bf2f(Are[d * NS + t]), ai = dt * bf2f(Aim[d * NS + t]);
      float br = dt * bf2f(Bre[d * NS + t]), bi = dt * bf2f(Bim[d * NS + t]);
      float cr = bf2f(Cre[d * NS + t]), ci = bf2f(Cim[d * NS + t]);
      pr[t * 12 + 0] = ar; pr[t * 12 + 1] = ai;
      pr[t * 12 + 2] = cr * br - ci * bi; pr[t * 12 + 3] = cr * bi + ci * br;
#pragma unroll
      for (int s = 0; s < 4; ++s) {
        float st = (float)(1 << s);
        float e = __expf(ar * st);
        float sn, cs;
        fast_cis(ai * st, sn, cs);
        pr[t * 12 + 4 + 2 * s] = e * cs; pr[t * 12 + 5 + 2 * s] = e * sn;
      }
    }
    __syncthreads();
    if (lane < 41) {
      int c_ = lane, seg, lv0;
      if (c_ < 16) { seg = 0; lv0 = 16 * c_; }
      else if (c_ < 24) { seg = 1; lv0 = 256 + 32 * (c_ - 16); }
      else if (c_ < 32) { seg = 2; lv0 = 512 + 64 * (c_ - 24); }
      else { seg = 3; lv0 = 1024 + 128 * (c_ - 32); }
      const float flv = (float)lv0;
      float acc[16];
#pragma unroll
      for (int i = 0; i < 16; ++i) acc[i] = 0.f;
      for (int n = wv * 32; n < wv * 32 + 32; ++n) {
        f32x4 p0 = *reinterpret_cast<const f32x4*>(&pr[n * 12]);
        f32x2 E = *reinterpret_cast<const f32x2*>(&pr[n * 12 + 4 + 2 * seg]);
        f32x2 En = {-E.y, E.x};
        float e = __expf(p0.x * flv);
        float sn, cs;
        fast_cis(p0.y * flv, sn, cs);
        float pr_ = e * cs, pi_ = e * sn;
        f32x2 S = {p0.z * pr_ - p0.w * pi_, p0.z * pi_ + p0.w * pr_};
#pragma unroll
        for (int i = 0; i < 16; ++i) {
          acc[i] += S.x;
          f32x2 sx = {S.x, S.x}, sy = {S.y, S.y};
          S = sx * E + sy * En;
        }
      }
#pragma unroll
      for (int i = 0; i < 16; ++i) red[wv * 656 + c_ * 16 + i] = acc[i];
    }
    __syncthreads();
    for (int i = t; i < 641; i += 256) Kd[d * KDS + i] = red[i] + red[656 + i] + red[2 * 656 + i] + red[3 * 656 + i];
  }
}

constexpr int HCS = 2192;
constexpr int HOFF = 2048;
constexpr int KROW = 72;

__global__ __launch_bounds__(256) void k1_conv(const u16* __restrict__ xnT, const u16* __restrict__ xT,
                                                const float* __restrict__ mod, const float* __restrict__ Kd,
                                                const u16* __restrict__ Dskip, u16* __restrict__ x1T) {
  __shared__ __attribute__((aligned(16))) u16 hc[8 * HCS];
  __shared__ __attribute__((aligned(16))) u16 kp[2 * 64 * KROW];
  __shared__ __attribute__((aligned(16))) u16 hr[HCS + 16];
  const int d = blockIdx.x, t = threadIdx.x, lane = t & 63, wv = t >> 6;
  const float ca1 = rbf(1.f + mod[D + d]), sa = mod[d], ga = mod[2 * D + d];
  u16 hv[8];
  {
    bf16x8 xn = *reinterpret_cast<const bf16x8*>(xnT + (size_t)d * L + 8 * t);
#pragma unroll
    for (int j = 0; j < 8; ++j) hv[j] = f2bf(rbf(bf2f((u16)xn[j]) * ca1) + sa);
  }
  {
    float kv[8];
#pragma unroll
    for (int j = 0; j < 8; ++j) kv[j] = Kd[d * KDS + kidx_of(8 * t + j)];
    u16 hi[8], lo[8];
#pragma unroll
    for (int j = 0; j < 8; ++j) { hi[j] = f2bf(kv[j]); lo[j] = f2bf(kv[j] - bf2f(hi[j])); }
    int l0 = 8 * t;
    *reinterpret_cast<bf16x8*>(&kp[(32 + (l0 >> 6)) * KROW + (l0 & 63)]) = *reinterpret_cast<bf16x8*>(hi);
    *reinterpret_cast<bf16x8*>(&kp[(96 + (l0 >> 6)) * KROW + (l0 & 63)]) = *reinterpret_cast<bf16x8*>(lo);
  }
  for (int i = t; i < 32 * KROW / 8; i += 256) {
    *reinterpret_cast<bf16x8*>(&kp[i * 8]) = bf16x8{0, 0, 0, 0, 0, 0, 0, 0};
    *reinterpret_cast<bf16x8*>(&kp[64 * KROW + i * 8]) = bf16x8{0, 0, 0, 0, 0, 0, 0, 0};
  }
  {
    u16 rv[8];
#pragma unroll
    for (int j = 0; j < 8; ++j) rv[j] = hv[7 - j];
    int i0 = 8 + HOFF - 8 * t - 7;
#pragma unroll
    for (int j = 0; j < 8; ++j) hr[i0 + j] = rv[j];
  }
  if (t < 8) hr[t] = 0;
  if (t == 8) hr[8] = 0;
  for (int i = 8 + HOFF + 1 + t; i < HCS + 16; i += 256) hr[i] = 0;
  __syncthreads();
  for (int u = t; u < HCS / 8; u += 256) {
    bf16x8 a0 = *reinterpret_cast<const bf16x8*>(&hr[8 * u]);
    bf16x8 a1 = *reinterpret_cast<const bf16x8*>(&hr[8 * u + 8]);
    u16 w[16];
#pragma unroll
    for (int j = 0; j < 8; ++j) { w[j] = (u16)a0[j]; w[8 + j] = (u16)a1[j]; }
#pragma unroll
    for (int c_ = 0; c_ < 8; ++c_) {
      bf16x8 o;
#pragma unroll
      for (int j = 0; j < 8; ++j) o[j] = (short)w[8 + j - c_];
      *reinterpret_cast<bf16x8*>(&hc[c_ * HCS + 8 * u]) = o;
    }
  }
  __syncthreads();
  const int n = lane & 15, q = lane >> 4, nh = n >> 3, cc = n & 7;
  f32x4 acc[2][4];
#pragma unroll
  for (int m = 0; m < 2; ++m)
#pragma unroll
    for (int j = 0; j < 4; ++j) acc[m][j] = f32x4{0.f, 0.f, 0.f, 0.f};
  for (int dl = wv; dl < 32; dl += 4) {
#pragma unroll
    for (int ks = 0; ks < 2; ++ks) {
      bf16x8 bfr[4];
#pragma unroll
      for (int nt = 0; nt < 4; ++nt) {
        int base = dl * 64 + nt * 16 - ks * 32 - 8 * q;
        int x0 = HOFF - base - 8 * nh;
        bfr[nt] = *reinterpret_cast<const bf16x8*>(&hc[cc * HCS + x0]);
      }
      {
        int row = 32 + 16 + n - dl;
        bf16x8 ah = *reinterpret_cast<const bf16x8*>(&kp[row * KROW + ks * 32 + 8 * q]);
        bf16x8 al = *reinterpret_cast<const bf16x8*>(&kp[(64 + row) * KROW + ks * 32 + 8 * q]);
#pragma unroll
        for (int nt = 0; nt < 4; ++nt) {
          acc[1][nt] = __builtin_amdgcn_mfma_f32_16x16x32_bf16(ah, bfr[nt], acc[1][nt], 0, 0, 0);
          acc[1][nt] = __builtin_amdgcn_mfma_f32_16x16x32_bf16(al, bfr[nt], acc[1][nt], 0, 0, 0);
        }
      }
      if (dl < 16) {
        int row = 32 + n - dl;
        bf16x8 ah = *reinterpret_cast<const bf16x8*>(&kp[row * KROW + ks * 32 + 8 * q]);
        bf16x8 al = *reinterpret_cast<const bf16x8*>(&kp[(64 + row) * KROW + ks * 32 + 8 * q]);
#pragma unroll
        for (int nt = 0; nt < 4; ++nt) {
          acc[0][nt] = __builtin_amdgcn_mfma_f32_16x16x32_bf16(ah, bfr[nt], acc[0][nt], 0, 0, 0);
          acc[0][nt] = __builtin_amdgcn_mfma_f32_16x16x32_bf16(al, bfr[nt], acc[0][nt], 0, 0, 0);
        }
      }
    }
  }
  __syncthreads();
  float* red = reinterpret_cast<float*>(hc);
#pragma unroll
  for (int m = 0; m < 2; ++m)
#pragma unroll
    for (int nt = 0; nt < 4; ++nt)
#pragma unroll
      for (int r = 0; r < 4; ++r) red[wv * L + (m * 16 + q * 4 + r) * 64 + nt * 16 + n] = acc[m][nt][r];
  __syncthreads();
  const float dsk = bf2f(Dskip[d]);
  {
    bf16x8 xr = *reinterpret_cast<const bf16x8*>(xT + (size_t)d * L + 8 * t);
    u16 o[8];
#pragma unroll
    for (int j = 0; j < 8; ++j) {
      int l = 8 * t + j;
      float y = rbf(red[l] + red[L + l] + red[2 * L + l] + red[3 * L + l]);
      float ya = rbf(y + rbf(bf2f(hv[j]) * dsk));
      o[j] = f2bf(bf2f((u16)xr[j]) + rbf(ga * ya));
    }
    *reinterpret_cast<bf16x8*>(x1T + (size_t)d * L + 8 * t) = *reinterpret_cast<bf16x8*>(o);
  }
}

__global__ __launch_bounds__(256) void k1b_ln2(const u16* __restrict__ x1T, const float* __restrict__ mod,
                                                u16* __restrict__ x1, u16* __restrict__ h2) {
  __shared__ __attribute__((aligned(16))) u16 tl[8 * 584];
  const int t = threadIdx.x, lane = t & 63, wv = t >> 6;
  const int r0 = blockIdx.x * 8;
  for (int dch = t; dch < D; dch += 256) {
    bf16x8 v = *reinterpret_cast<const bf16x8*>(x1T + (size_t)dch * L + r0);
#pragma unroll
    for (int j = 0; j < 8; ++j) tl[j * 584 + dch] = (u16)v[j];
  }
  __syncthreads();
  for (int rr = wv * 2; rr < wv * 2 + 2; ++rr) {
    float v[9];
    float s = 0.f;
#pragma unroll
    for (int j = 0; j < 9; ++j) { v[j] = bf2f(tl[rr * 584 + lane + 64 * j]); s += v[j]; }
    s = wave_sum(s);
    float m = s * (1.f / D);
    float q = 0.f;
#pragma unroll
    for (int j = 0; j < 9; ++j) { float dd = v[j] - m; q += dd * dd; }
    q = wave_sum(q);
    float var = rbf(q * (1.f / D));
    float sq = rbf(sqrtf(rbf(var + 1e-5f)));
    float mb = rbf(m);
    size_t ro = (size_t)(r0 + rr) * D;
#pragma unroll
    for (int j = 0; j < 9; ++j) {
      int k = lane + 64 * j;
      float hv = rbf(rbf(rbf(v[j] - mb) / sq) * rbf(1.f + mod[4 * D + k])) + mod[3 * D + k];
      h2[ro + k] = f2bf(hv);
      x1[ro + k] = f2bf(v[j]);
    }
  }
}

constexpr int G1_BM = 128, G1_BN = 288;
constexpr int G1_ABYTES = G1_BM * 64 * 2, G1_BBYTES = 64 * G1_BN * 2;

constexpr int G1_NST = 3;
__global__ __launch_bounds__(512) void k2_gemm1(const u16* __restrict__ h2, const u16* __restrict__ w1,
                                                 u16* __restrict__ hmid) {
  __shared__ __attribute__((aligned(16))) char lds[G1_NST * (G1_ABYTES + G1_BBYTES)];
  const int t = threadIdx.x, lane = t & 63, wv = t >> 6;
  const int xcd = blockIdx.x & 7, jj = blockIdx.x >> 3;
  const int bn = 2 * xcd + (jj & 1), bm = jj >> 1;
  const int m0 = bm * G1_BM, n0 = bn * G1_BN;
  const int wm = wv & 3, wn = wv >> 2;
  const int n = lane & 15, q = lane >> 4;
  f32x4 acc[2][9];
#pragma unroll
  for (int i = 0; i < 2; ++i)
#pragma unroll
    for (int j = 0; j < 9; ++j) acc[i][j] = f32x4{0.f, 0.f, 0.f, 0.f};

  int aoff[2];
#pragma unroll
  for (int i = 0; i < 2; ++i) {
    int o = (wv * 2 + i) * 1024 + lane * 16;
    int row = o >> 7, cp = (o >> 4) & 7;
    aoff[i] = row * D + ((cp ^ (row & 7)) * 8);
  }
  int boff[5], bdst[5];
#pragma unroll
  for (int j = 0; j < 5; ++j) {
    int idx = wv + 8 * j;
    if (idx >= 36) idx -= 8;
    int o = idx * 1024 + lane * 16;
    int row = o / 576, cb = o - row * 576;
    boff[j] = row * F + ((cb / 2) ^ (((row >> 3) & 1) << 4));
    bdst[j] = idx * 1024;
  }
  const u16* abase = h2 + (size_t)m0 * D;
  const u16* bbase = w1 + n0;
  auto stage = [&](int kt, int buf) {
    char* al = lds + buf * (G1_ABYTES + G1_BBYTES);
    char* bl = al + G1_ABYTES;
#pragma unroll
    for (int i = 0; i < 2; ++i)
      __builtin_amdgcn_global_load_lds((const unsigned*)(abase + kt * 64 + aoff[i]),
                                       (unsigned*)(al + (wv * 2 + i) * 1024), 16, 0, 0);
#pragma unroll
    for (int j = 0; j < 5; ++j)
      __builtin_amdgcn_global_load_lds((const unsigned*)(bbase + (size_t)kt * 64 * F + boff[j]),
                                       (unsigned*)(bl + bdst[j]), 16, 0, 0);
  };
  stage(0, 0);
  stage(1, 1);
  int buf = 0;
  for (int kt = 0; kt < 9; ++kt) {
    if (kt < 8) asm volatile("s_waitcnt vmcnt(7)" ::: "memory");
    else asm volatile("s_waitcnt vmcnt(0)" ::: "memory");
    __builtin_amdgcn_s_barrier();
    if (kt + 2 < 9) stage(kt + 2, buf == 0 ? 2 : buf - 1);
    const char* al = lds + buf * (G1_ABYTES + G1_BBYTES);
    const u16* bl = reinterpret_cast<const u16*>(al + G1_ABYTES);
#pragma unroll
    for (int ks = 0; ks < 2; ++ks) {
      bf16x8 af[2];
#pragma unroll
      for (int mt = 0; mt < 2; ++mt) {
        int row = wm * 32 + mt * 16 + n;
        af[mt] = *reinterpret_cast<const bf16x8*>(al + row * 128 + (((ks * 4 + q) ^ (row & 7)) * 16));
      }
      s16x4 lo[9], hi[9];
      {
        uint32_t ab = lds_addr(bl + (ks * 32 + 8 * q + (n >> 2)) * G1_BN + wn * 144 + 4 * (n & 3));
        int sg = (q & 1) * 32;
        if (wn & 1) sg = -sg;
        TrLoop<0, G1_BN * 2>::run(ab + sg, ab - sg, lo, hi);
      }
      asm volatile("s_waitcnt lgkmcnt(0)" ::: "memory");
      __builtin_amdgcn_sched_barrier(0);
#pragma unroll
      for (int nt = 0; nt < 9; ++nt) {
        bf16x8 bfr = {lo[nt][0], lo[nt][1], lo[nt][2], lo[nt][3], hi[nt][0], hi[nt][1], hi[nt][2], hi[nt][3]};
#pragma unroll
        for (int mt = 0; mt < 2; ++mt)
          acc[mt][nt] = __builtin_amdgcn_mfma_f32_16x16x32_bf16(af[mt], bfr, acc[mt][nt], 0, 0, 0);
      }
    }
    buf = (buf == 2) ? 0 : buf + 1;
  }
  __syncthreads();
  u16* ct = reinterpret_cast<u16*>(lds);
  constexpr int CS = 296;
#pragma unroll
  for (int mt = 0; mt < 2; ++mt)
#pragma unroll
    for (int nt = 0; nt < 9; ++nt)
#pragma unroll
      for (int r = 0; r < 4; ++r) {
        float hv = rbf(acc[mt][nt][r]);
        float sv = hv / (1.f + __expf(-hv));
        ct[(wm * 32 + mt * 16 + q * 4 + r) * CS + wn * 144 + nt * 16 + n] = f2bf(sv);
      }
  __syncthreads();
  for (int ch = t; ch < 128 * 36; ch += 512) {
    int row = ch / 36, cc = ch - row * 36;
    *reinterpret_cast<bf16x8*>(hmid + (size_t)(m0 + row) * F + n0 + cc * 8) =
        *reinterpret_cast<const bf16x8*>(&ct[row * CS + cc * 8]);
  }
}

constexpr int G2_BN = 144, G2_SPLIT = 4;
constexpr int G2_BBYTES = 64 * G2_BN * 2;

constexpr int G2_NST = 4;
__global__ __launch_bounds__(512) void k3_gemm2(const u16* __restrict__ hmid, const u16* __restrict__ w2,
                                                 float* __restrict__ part) {
  __shared__ __attribute__((aligned(16))) char lds[G2_NST * (G1_ABYTES + G2_BBYTES)];
  const int t = threadIdx.x, lane = t & 63, wv = t >> 6;
  const int xcd = blockIdx.x & 7, jj = blockIdx.x >> 3;
  const int pr_ = 2 * xcd + (jj & 1);
  const int sp = pr_ >> 2, bn = pr_ & 3, bm = jj >> 1;
  const int m0 = bm * 128, n0 = bn * G2_BN, k0 = sp * (F / G2_SPLIT);
  const int wm = wv & 3, ks = wv >> 2;
  const int n = lane & 15, q = lane >> 4;
  f32x4 acc[2][9];
#pragma unroll
  for (int i = 0; i < 2; ++i)
#pragma unroll
    for (int j = 0; j < 9; ++j) acc[i][j] = f32x4{0.f, 0.f, 0.f, 0.f};
  int aoff[2];
#pragma unroll
  for (int i = 0; i < 2; ++i) {
    int o = (wv * 2 + i) * 1024 + lane * 16;
    int row = o >> 7, cp = (o >> 4) & 7;
    aoff[i] = row * F + ((cp ^ (row & 7)) * 8);
  }
  int boff[3], bdst[3];
#pragma unroll
  for (int j = 0; j < 3; ++j) {
    int idx = wv + 8 * j;
    if (idx >= 18) idx -= 8;
    int o = idx * 1024 + lane * 16;
    int row = o / 288, cb = o - row * 288;
    boff[j] = row * D + ((cb < 256) ? ((cb / 2) ^ (((row >> 3) & 1) << 4)) : cb / 2);
    bdst[j] = idx * 1024;
  }
  const u16* abase = hmid + (size_t)m0 * F + k0;
  const u16* bbase = w2 + (size_t)k0 * D + n0;
  auto stage = [&](int kt, int buf) {
    char* al = lds + buf * (G1_ABYTES + G2_BBYTES);
    char* bl = al + G1_ABYTES;
#pragma unroll
    for (int i = 0; i < 2; ++i)
      __builtin_amdgcn_global_load_lds((const unsigned*)(abase + kt * 64 + aoff[i]),
                                       (unsigned*)(al + (wv * 2 + i) * 1024), 16, 0, 0);
#pragma unroll
    for (int j = 0; j < 3; ++j)
      __builtin_amdgcn_global_load_lds((const unsigned*)(bbase + (size_t)kt * 64 * D + boff[j]),
                                       (unsigned*)(bl + bdst[j]), 16, 0, 0);
  };
  constexpr int NKT = F / G2_SPLIT / 64;
  stage(0, 0);
  stage(1, 1);
  stage(2, 2);
  for (int kt = 0; kt < NKT; ++kt) {
    if (kt < NKT - 2) asm volatile("s_waitcnt vmcnt(10)" ::: "memory");
    else if (kt == NKT - 2) asm volatile("s_waitcnt vmcnt(5)" ::: "memory");
    else asm volatile("s_waitcnt vmcnt(0)" ::: "memory");
    __builtin_amdgcn_s_barrier();
    if (kt + 3 < NKT) stage(kt + 3, (kt + 3) & 3);
    const char* al = lds + (kt & 3) * (G1_ABYTES + G2_BBYTES);
    const u16* bl = reinterpret_cast<const u16*>(al + G1_ABYTES);
    {
      bf16x8 af[2];
#pragma unroll
      for (int mt = 0; mt < 2; ++mt) {
        int row = wm * 32 + mt * 16 + n;
        af[mt] = *reinterpret_cast<const bf16x8*>(al + row * 128 + (((ks * 4 + q) ^ (row & 7)) * 16));
      }
      s16x4 lo[9], hi[9];
      {
        uint32_t ab = lds_addr(bl + (ks * 32 + 8 * q + (n >> 2)) * G2_BN + 4 * (n & 3));
        int sg = (q & 1) * 32;
        TrLoop<0, G2_BN * 2>::run(ab + sg, ab - sg, lo, hi);
      }
      asm volatile("s_waitcnt lgkmcnt(0)" ::: "memory");
      __builtin_amdgcn_sched_barrier(0);
#pragma unroll
      for (int nt = 0; nt < 9; ++nt) {
        bf16x8 bfr = {lo[nt][0], lo[nt][1], lo[nt][2], lo[nt][3], hi[nt][0], hi[nt][1], hi[nt][2], hi[nt][3]};
#pragma unroll
        for (int mt = 0; mt < 2; ++mt)
          acc[mt][nt] = __builtin_amdgcn_mfma_f32_16x16x32_bf16(af[mt], bfr, acc[mt][nt], 0, 0, 0);
      }
    }
  }
  __syncthreads();
  float* red = reinterpret_cast<float*>(lds);
  for (int half = 0; half < 2; ++half) {
    if (ks == 1 && (wm >> 1) == half) {
#pragma unroll
      for (int mt = 0; mt < 2; ++mt)
#pragma unroll
        for (int nt = 0; nt < 9; ++nt)
#pragma unroll
          for (int r = 0; r < 4; ++r)
            red[((wm & 1) * 32 + mt * 16 + q * 4 + r) * 144 + nt * 16 + n] = acc[mt][nt][r];
    }
    __syncthreads();
    if (ks == 0 && (wm >> 1) == half) {
#pragma unroll
      for (int mt = 0; mt < 2; ++mt)
#pragma unroll
        for (int nt = 0; nt < 9; ++nt)
#pragma unroll
          for (int r = 0; r < 4; ++r)
            part[((size_t)sp * L + m0 + wm * 32 + mt * 16 + q * 4 + r) * D + n0 + nt * 16 + n] =
                acc[mt][nt][r] + red[((wm & 1) * 32 + mt * 16 + q * 4 + r) * 144 + nt * 16 + n];
    }
    __syncthreads();
  }
}

__global__ __launch_bounds__(256) void k4_final(const float* __restrict__ part, const u16* __restrict__ x1,
                                                 const float* __restrict__ mod, u16* __restrict__ out) {
  int i = blockIdx.x * 256 + threadIdx.x;
  int e = i * 4;
  if (e >= L * D) return;
  float4 s = *reinterpret_cast<const float4*>(part + e);
#pragma unroll
  for (int p = 1; p < G2_SPLIT; ++p) {
    float4 v = *reinterpret_cast<const float4*>(part + (size_t)p * L * D + e);
    s.x += v.x; s.y += v.y; s.z += v.z; s.w += v.w;
  }
  int dcol = e % D;
  float sv[4] = {s.x, s.y, s.z, s.w};
  u16 o[4];
#pragma unroll
  for (int j = 0; j < 4; ++j) {
    float gb = mod[5 * D + dcol + j];
    o[j] = f2bf(bf2f(x1[e + j]) + rbf(gb * rbf(sv[j])));
  }
  *reinterpret_cast<uint2*>(out + e) = make_uint2(o[0] | ((uint32_t)o[1] << 16), o[2] | ((uint32_t)o[3] << 16));
}

extern "C" void kernel_launch(void* const* d_in, const int* in_sizes, int n_in, void* d_out, int out_size,
                              void* d_ws, size_t ws_size, hipStream_t stream) {
  const u16* A_u = (const u16*)d_in[0];
  const u16* Are = (const u16*)d_in[1];
  const u16* Aim = (const u16*)d_in[2];
  const u16* Bre = (const u16*)d_in[3];
  const u16* Bim = (const u16*)d_in[4];
  const u16* Cre = (const u16*)d_in[5];
  const u16* Cim = (const u16*)d_in[6];
  const u16* logdt = (const u16*)d_in[7];
  const u16* Dskip = (const u16*)d_in[8];
  const u16* w1 = (const u16*)d_in[10];
  const u16* w2 = (const u16*)d_in[11];
  const u16* c = (const u16*)d_in[12];
  const u16* wmod = (const u16*)d_in[13];
  char* ws = (char*)d_ws;
  float* mod = (float*)ws;                         ws += 16384;
  float* Kd = (float*)ws;                          ws += (size_t)D * KDS * 4;
  u16* xnT = (u16*)ws;                             ws += (size_t)L * D * 2;
  u16* xT = (u16*)ws;                              ws += (size_t)L * D * 2;
  u16* x1T = (u16*)ws;                             ws += (size_t)L * D * 2;
  u16* x1 = (u16*)ws;                              ws += (size_t)L * D * 2;
  u16* h2 = (u16*)ws;                              ws += (size_t)L * D * 2;
  u16* hmid = (u16*)ws;                            ws += (size_t)L * F * 2;
  float* part = (float*)ws;
  k0_prep<<<K0_MOD_BLOCKS + K0_STAT_BLOCKS + D, 256, 0, stream>>>(c, wmod, A_u, Are, Aim, Bre, Bim, Cre, Cim, logdt,
                                                                  mod, xnT, xT, Kd);
  k1_conv<<<D, 256, 0, stream>>>(xnT, xT, mod, Kd, Dskip, x1T);
  k1b_ln2<<<L / 8, 256, 0, stream>>>(x1T, mod, x1, h2);
  k2_gemm1<<<256, 512, 0, stream>>>(h2, w1, hmid);
  k3_gemm2<<<256, 512, 0, stream>>>(hmid, w2, part);
  k4_final<<<(L * D / 4 + 255) / 256, 256, 0, stream>>>(part, x1, mod, (u16*)d_out);
}
```
